```python
import torch

def _softmax(x, axis=-1):
    return torch.softmax(x, dim=axis)
def _gelu(x):
    return torch.nn.functional.gelu(x, approximate="tanh")
def _silu(x):
    return torch.nn.functional.silu(x)
def _sigmoid(x):
    return torch.sigmoid(x)
def _layernorm(x, axis=-1, eps=1e-5):
    m = x.mean(dim=axis, keepdim=True)
    v = x.var(dim=axis, keepdim=True, unbiased=False)
    return (x - m) / torch.sqrt(v + eps)
def _scatter_add(tgt, idx, src, axis=0):
    return tgt.clone().index_add_(axis, idx, src)
def _segment_sum(src, seg, n):
    out = torch.zeros((n,) + src.shape[1:], dtype=src.dtype)
    return out.index_add_(0, seg, src)
def _pad(x, pw):
    flat = tuple(v for pair in reversed(pw) for v in pair)
    return torch.nn.functional.pad(x, flat)

kernel_name = 'concat_heads__ssm_conv_h3__sliding_block__prompt'

def setup_inputs():

    _w = (lambda a: (lambda t: t.to(torch.bfloat16) if t.is_floating_point() else t)(torch.as_tensor(a)))
    def _A_setup():
        B, S, D = 1, 2048, 640
        ks = 4
        return {
            "q": _w((lambda *s: torch.randn(*s, dtype=torch.bfloat16) * 0.05)(B, S, D)),
            "k": _w((lambda *s: torch.randn(*s, dtype=torch.bfloat16) * 0.8)(B, S, D)),
            "v": _w((lambda *s: torch.randn(*s, dtype=torch.bfloat16) * 0.8)(B, S, D)),
            "shift_k": _w((lambda *s: torch.randn(*s, dtype=torch.bfloat16) * 0.8)(D, ks) * 0.5),
            "ssm_k": _w((lambda *s: torch.randn(*s, dtype=torch.bfloat16) * 3.2)(D, S) * (1.0 / S ** 0.5)),
            "ssm_D": _w((lambda *s: torch.randn(*s, dtype=torch.bfloat16) * 0.8)(D)),
        }
    _A_in = _A_setup()

    def _B_setup():
        B, S, D = 1, 2048, 640
        return {
            "x": _w((lambda *s: torch.randn(*s, dtype=torch.bfloat16) * 0.2)(B, S, D)),
            "wqkv": _w((lambda *s: torch.randn(*s, dtype=torch.bfloat16) * 0.2)(D, 3 * D)),
            "wo": _w((lambda *s: torch.randn(*s, dtype=torch.bfloat16) * 0.05)(D, D)),
        }
    _B_in = _B_setup()

    return {
        "A_q": _A_in["q"],
        "A_k": _A_in["k"],
        "A_v": _A_in["v"],
        "A_shift_k": _A_in["shift_k"],
        "A_ssm_k": _A_in["ssm_k"],
        "A_ssm_D": _A_in["ssm_D"],
        "B_x": _B_in["x"],
        "B_wqkv": _B_in["wqkv"],
        "B_wo": _B_in["wo"],
    }

def reference(A_q, A_k, A_v, A_shift_k, A_ssm_k, A_ssm_D, B_x, B_wqkv, B_wo):
    def _A_ref(q, k, v, shift_k, ssm_k, ssm_D):
        B, S, D = q.shape
        ks = shift_k.shape[-1]
        k_pad = _pad(k, ((0, 0), (ks - 1, 0), (0, 0)))
        k_shift = torch.zeros_like(k)
        for t in range(ks):
            k_shift = k_shift + k_pad[:, t:t + S, :] * shift_k[:, ks - 1 - t][None, None, :]
        kv = k_shift * v
        L = 2 * S
        kv_pad = _pad(kv, ((0, 0), (0, S), (0, 0)))
        kf_pad = _pad(ssm_k, ((0, 0), (0, S)))
        kv_f = (lambda x, axes=(-2,-1): torch.fft.rfft2(x.float(), dim=axes))(kv_pad[:, None, :, :], axes=(1, 2))
        kf_f = (lambda x, axes=(-2,-1): torch.fft.rfft2(x.float(), dim=axes))(kf_pad[:, None, :], axes=(1, 2))
        kf_f = (lambda x, *d: x.permute(*d))(kf_f, 1, 2, 0)
        y_f = kv_f * kf_f[None, :, :, :]
        y = (lambda x, s, axes=(-2,-1): torch.fft.irfft2(x, s=s, dim=axes).to(torch.bfloat16))(y_f, (1, L), axes=(1, 2))[:, 0, :S, :]
        y = y + kv * ssm_D[None, None, :]
        return q * y

    def _B_ref(x, wqkv, wo):
        B, S, D = x.shape
        H = 32; Dh = D // H; W = 256
        qkv = (lambda x, *d: x.permute(*d))((x @ wqkv).reshape(B, S, 3, H, Dh), 2, 0, 3, 1, 4)
        q, k, v = qkv[0], qkv[1], qkv[2]
        scores = torch.einsum("bhqd,bhkd->bhqk", q, k) * (Dh ** -0.5)
        idx = torch.arange(S)
        mask = (idx[:, None] - idx[None, :] >= 0) & (idx[:, None] - idx[None, :] < W)
        scores = torch.where(mask[None, None], scores, -1e9)
        p = _softmax(scores, axis=-1)
        o = torch.einsum("bhqk,bhkd->bhqd", p, v)
        return (lambda x, *d: x.permute(*d))(o, 0, 2, 1, 3).reshape(B, S, D) @ wo

    a = _A_ref(A_q, A_k, A_v, A_shift_k, A_ssm_k, A_ssm_D)
    b = _B_ref(B_x, B_wqkv, B_wo)
    return torch.concatenate([a, b], axis=-1)
```

```cpp
#include <hip/hip_runtime.h>
#include <hip/hip_bf16.h>
#include <stdint.h>
typedef unsigned short u16;
__device__ __forceinline__ float bf2f(u16 x){ return __uint_as_float(((unsigned)x)<<16); }
__device__ __forceinline__ u16 f2bf(float f){ __bf16 b = (__bf16)f; return __builtin_bit_cast(u16,b); }
__device__ __forceinline__ float rbf(float f){ return bf2f(f2bf(f)); }
constexpr int S=2048, D=640, H=32, DH=20, W=256;

__global__ void k_kv(const u16* k,const u16* v,const u16* sk,u16* kvT){
  int i = blockIdx.x*blockDim.x+threadIdx.x; if(i>=S*D) return;
  int s=i/D, d=i%D;
  float acc=0.f;
  for(int t=0;t<4;t++){ int ss=s+t-3; float kk = ss>=0? bf2f(k[ss*D+d]):0.f; float p=rbf(kk*bf2f(sk[d*4+3-t])); acc=rbf(acc+p);}
  kvT[d*S+s]=f2bf(acc*bf2f(v[i]));
}
__global__ void k_conv(const u16* kvT,const u16* h,const u16* Dv,const u16* q,u16* out){
  int d=blockIdx.x; __shared__ float xs[S], hs[S];
  for(int i=threadIdx.x;i<S;i+=blockDim.x){xs[i]=bf2f(kvT[d*S+i]);hs[i]=bf2f(h[d*S+i]);}
  __syncthreads();
  for(int s=threadIdx.x;s<S;s+=blockDim.x){
    float acc=0.f; for(int j=0;j<=s;j++) acc+=hs[j]*xs[s-j];
    float y=rbf(acc); y=rbf(y+rbf(xs[s]*bf2f(Dv[d])));
    out[s*1280+d]=f2bf(bf2f(q[s*D+d])*y);
  }
}
__global__ void k_gemm(const u16* A,const u16* B,u16* C,int N,int K,int ldc){
  int n=blockIdx.x*blockDim.x+threadIdx.x, m=blockIdx.y; if(n>=N) return;
  float acc=0.f; for(int k=0;k<K;k++) acc+=bf2f(A[m*K+k])*bf2f(B[k*N+n]);
  C[m*ldc+n]=f2bf(acc);
}
__global__ void k_attn(const u16* qkv,u16* o){
  int s=blockIdx.x, h=blockIdx.y; int t=threadIdx.x;
  __shared__ float p[W]; __shared__ float red[W];
  int key=s-t; float sc=-1e30f;
  if(key>=0){ float a=0; for(int d=0;d<DH;d++) a+=bf2f(qkv[s*1920+h*DH+d])*bf2f(qkv[key*1920+640+h*DH+d]); sc=a*0.22360679775f; }
  red[t]=sc; __syncthreads();
  for(int o2=128;o2>0;o2>>=1){ if(t<o2) red[t]=fmaxf(red[t],red[t+o2]); __syncthreads(); }
  float m=red[0]; __syncthreads();
  float e= key>=0? __expf(sc-m):0.f; red[t]=e; __syncthreads();
  for(int o2=128;o2>0;o2>>=1){ if(t<o2) red[t]+=red[t+o2]; __syncthreads(); }
  float l=red[0]; p[t]=rbf(e/l); __syncthreads();
  if(t<DH){ float a=0; for(int j=0;j<W;j++){int kk=s-j; if(kk>=0) a+=p[j]*bf2f(qkv[kk*1920+1280+h*DH+t]);} o[s*640+h*DH+t]=f2bf(a);}
}
extern "C" void kernel_launch(void* const* d_in, const int* in_sizes, int n_in, void* d_out, int out_size, void* d_ws, size_t ws_size, hipStream_t stream) {
  const u16* q=(const u16*)d_in[0]; const u16* k=(const u16*)d_in[1]; const u16* v=(const u16*)d_in[2];
  const u16* sk=(const u16*)d_in[3]; const u16* h=(const u16*)d_in[4]; const u16* Dv=(const u16*)d_in[5];
  const u16* x=(const u16*)d_in[6]; const u16* wqkv=(const u16*)d_in[7]; const u16* wo=(const u16*)d_in[8];
  u16* out=(u16*)d_out; u16* ws=(u16*)d_ws;
  u16* kvT=ws; u16* qkv=ws+S*D; u16* o=qkv+S*1920;
  k_kv<<<(S*D+255)/256,256,0,stream>>>(k,v,sk,kvT);
  k_conv<<<D,256,0,stream>>>(kvT,h,Dv,q,out);
  k_gemm<<<dim3(1920/128,S),128,0,stream>>>(x,wqkv,qkv,1920,640,1920);
  k_attn<<<dim3(S,H),256,0,stream>>>(qkv,o);
  k_gemm<<<dim3(640/128,S),128,0,stream>>>(o,wo,out+640,640,640,1280);
}
```

```cpp
#include <hip/hip_runtime.h>
#include <hip/hip_bf16.h>
#include <stdint.h>

typedef unsigned short u16;
typedef unsigned int u32;
typedef __bf16 bf16x8 __attribute__((ext_vector_type(8)));
typedef __bf16 bf16x2 __attribute__((ext_vector_type(2)));
typedef float f32x4 __attribute__((ext_vector_type(4)));
typedef float f32x2 __attribute__((ext_vector_type(2)));
typedef u32 u32x2 __attribute__((ext_vector_type(2)));
typedef u32 u32x4 __attribute__((ext_vector_type(4)));
typedef short s16x4 __attribute__((ext_vector_type(4)));
#define LDS3 __attribute__((address_space(3)))
#define DI __device__ __forceinline__

constexpr int SEQ = 2048, DM = 640;
constexpr size_t PLANE = (size_t)SEQ * DM * 2;
constexpr size_t WS_XR = 0;
constexpr size_t WS_Q = 1 * PLANE;
constexpr size_t WS_K = 2 * PLANE;
constexpr size_t WS_V = 3 * PLANE;
constexpr size_t WS_O = 4 * PLANE;
constexpr size_t WS_Y2 = 5 * PLANE;
constexpr size_t WS_WO = 6 * PLANE;
constexpr float QSCALE = 0.22360679774997896f * 1.4426950408889634f;

DI float bf_lo(u32 v) { return __uint_as_float(v << 16); }
DI float bf_hi(u32 v) { return __uint_as_float(v & 0xffff0000u); }
DI u32 pack2(float lo, float hi) { f32x2 v = {lo, hi}; return __builtin_bit_cast(u32, __builtin_convertvector(v, bf16x2)); }
DI float rbf(float f) { return bf_lo(pack2(f, f)); }
DI f32x4 mfma16(u32x4 a, u32x4 b, f32x4 c) {
  return __builtin_amdgcn_mfma_f32_16x16x32_bf16(__builtin_bit_cast(bf16x8, a), __builtin_bit_cast(bf16x8, b), c, 0, 0, 0);
}
DI void glds16(const void* g, u32 lds_base) {
  u32 sv;
  asm volatile("s_mov_b32 %0, m0\n\ts_mov_b32 m0, %2\n\ts_nop 0\n\tglobal_load_lds_dwordx4 %1, off\n\ts_mov_b32 m0, %0"
               : "=&s"(sv) : "v"(g), "s"(lds_base) : "memory");
}
DI u32x4 lds_r128(u32 a) { return *(const LDS3 u32x4*)(uintptr_t)a; }
DI u32x2 lds_r64(u32 a) { return *(const LDS3 u32x2*)(uintptr_t)a; }
DI void lds_w128(u32 a, u32x4 v) { *(LDS3 u32x4*)(uintptr_t)a = v; }
DI void lds_w64(u32 a, u32x2 v) { *(LDS3 u32x2*)(uintptr_t)a = v; }
DI u32x2 lds_tr(u32 a) { return __builtin_bit_cast(u32x2, __builtin_amdgcn_ds_read_tr16_b64_v4i16((LDS3 s16x4*)(uintptr_t)a)); }
#define WAIT_BAR(N) asm volatile("s_waitcnt vmcnt(" #N ") lgkmcnt(0)\n\ts_barrier" ::: "memory")
#define UNR _Pragma("unroll")

constexpr int K1_STAGE = 32768, K1_KV = 4 * K1_STAGE, K1_LDS = K1_KV + 64 * 144;

DI void kv_compute(const u32 (&kk)[7], const u32 (&vv)[4], u32x4 sh, u32x2& o0, u32x2& o1) {
  float r0[4], r1[4];
  const float a0 = bf_lo(sh[0]), a1 = bf_hi(sh[0]), a2 = bf_lo(sh[1]), a3 = bf_hi(sh[1]);
  const float b0 = bf_lo(sh[2]), b1 = bf_hi(sh[2]), b2 = bf_lo(sh[3]), b3 = bf_hi(sh[3]);
  UNR for (int i = 0; i < 4; ++i) {
    u32 p = pack2(bf_lo(kk[i]) * a3, bf_hi(kk[i]) * b3);
    u32 q = pack2(bf_lo(kk[i + 1]) * a2, bf_hi(kk[i + 1]) * b2);
    p = pack2(bf_lo(p) + bf_lo(q), bf_hi(p) + bf_hi(q));
    q = pack2(bf_lo(kk[i + 2]) * a1, bf_hi(kk[i + 2]) * b1);
    p = pack2(bf_lo(p) + bf_lo(q), bf_hi(p) + bf_hi(q));
    q = pack2(bf_lo(kk[i + 3]) * a0, bf_hi(kk[i + 3]) * b0);
    p = pack2(bf_lo(p) + bf_lo(q), bf_hi(p) + bf_hi(q));
    r0[i] = bf_lo(p) * bf_lo(vv[i]);
    r1[i] = bf_hi(p) * bf_hi(vv[i]);
  }
  o0 = u32x2{pack2(r0[3], r0[2]), pack2(r0[1], r0[0])};
  o1 = u32x2{pack2(r1[3], r1[2]), pack2(r1[1], r1[0])};
}

template <bool SWAP>
DI void k1_gemm(const u16* __restrict__ x, const u16* __restrict__ wqkv, const u16* __restrict__ kin, const u16* __restrict__ vin,
                const u16* __restrict__ shk, const u16* __restrict__ wo, char* __restrict__ ws, u32 lds0, int rt, int ct, int task, int wofrag) {
  const int tid = threadIdx.x, lane = tid & 63, wid = __builtin_amdgcn_readfirstlane(tid >> 6);
  const int l15 = lane & 15, g = lane >> 4;
  const char* xs = (const char*)x + (size_t)(rt * 128 + wid * 16 + (lane >> 3)) * 1280 + (((lane & 7) ^ (lane >> 3)) * 16);
  const char* ws0 = (const char*)wqkv + (size_t)(wid * 8 + g) * 3840 + ct * 256 + ((l15 ^ ((g << 2) | ((2 * wid) & 3))) * 16);
  const char* ws1 = (const char*)wqkv + (size_t)(wid * 8 + 4 + g) * 3840 + ct * 256 + ((l15 ^ ((g << 2) | ((2 * wid + 1) & 3))) * 16);
  const u32 xd = lds0 + wid * 2048, wd = lds0 + 16384 + wid * 2048;
#define K1_ISSUE(kt) do { const u32 sb = ((kt) & 3) * K1_STAGE; \
    glds16(xs + (kt) * 128, xd + sb); glds16(xs + 8 * 1280 + (kt) * 128, xd + sb + 1024); \
    glds16(ws0 + (size_t)(kt) * 64 * 3840, wd + sb); glds16(ws1 + (size_t)(kt) * 64 * 3840, wd + sb + 1024); } while (0)
  K1_ISSUE(0); K1_ISSUE(1); K1_ISSUE(2);
  const int st = task / 10, cg = task % 10;
  const int cp = tid & 31, rg = tid >> 5;
  const int s0 = st * 64 + rg * 4, c0 = cg * 64 + cp * 2;
  u32 kk[7], vv[4]; u32x4 sh;
  {
    const char* kp = (const char*)kin + (size_t)s0 * 1280 + c0 * 2;
    const char* kq = (s0 == 0) ? kp : kp - 3 * 1280;
    asm volatile("global_load_dword %0, %1, off" : "=v"(kk[0]) : "v"(kq) : "memory");
    asm volatile("global_load_dword %0, %1, off offset:1280" : "=v"(kk[1]) : "v"(kq) : "memory");
    asm volatile("global_load_dword %0, %1, off offset:2560" : "=v"(kk[2]) : "v"(kq) : "memory");
    asm volatile("global_load_dword %0, %1, off" : "=v"(kk[3]) : "v"(kp) : "memory");
    asm volatile("global_load_dword %0, %1, off offset:1280" : "=v"(kk[4]) : "v"(kp) : "memory");
    asm volatile("global_load_dword %0, %1, off offset:2560" : "=v"(kk[5]) : "v"(kp) : "memory");
    asm volatile("global_load_dword %0, %1, off offset:3840" : "=v"(kk[6]) : "v"(kp) : "memory");
    const char* vp = (const char*)vin + (size_t)s0 * 1280 + c0 * 2;
    asm volatile("global_load_dword %0, %1, off" : "=v"(vv[0]) : "v"(vp) : "memory");
    asm volatile("global_load_dword %0, %1, off offset:1280" : "=v"(vv[1]) : "v"(vp) : "memory");
    asm volatile("global_load_dword %0, %1, off offset:2560" : "=v"(vv[2]) : "v"(vp) : "memory");
    asm volatile("global_load_dword %0, %1, off offset:3840" : "=v"(vv[3]) : "v"(vp) : "memory");
    const char* sp = (const char*)shk + c0 * 8;
    asm volatile("global_load_dwordx4 %0, %1, off" : "=v"(sh) : "v"(sp) : "memory");
  }
  u32 wf[8];
  {
    const int f = wofrag < 800 ? wofrag : 0;
    const char* wp = (const char*)wo + (size_t)((f % 20) * 32 + 8 * g) * 1280 + ((f / 20) * 16 + l15) * 2;
    const char* wq = wp + 4 * 1280;
    asm volatile("global_load_ushort %0, %1, off" : "=v"(wf[0]) : "v"(wp) : "memory");
    asm volatile("global_load_ushort %0, %1, off offset:1280" : "=v"(wf[1]) : "v"(wp) : "memory");
    asm volatile("global_load_ushort %0, %1, off offset:2560" : "=v"(wf[2]) : "v"(wp) : "memory");
    asm volatile("global_load_ushort %0, %1, off offset:3840" : "=v"(wf[3]) : "v"(wp) : "memory");
    asm volatile("global_load_ushort %0, %1, off" : "=v"(wf[4]) : "v"(wq) : "memory");
    asm volatile("global_load_ushort %0, %1, off offset:1280" : "=v"(wf[5]) : "v"(wq) : "memory");
    asm volatile("global_load_ushort %0, %1, off offset:2560" : "=v"(wf[6]) : "v"(wq) : "memory");
    asm volatile("global_load_ushort %0, %1, off offset:3840" : "=v"(wf[7]) : "v"(wq) : "memory");
  }
  const int wm = wid >> 2, wn = wid & 3;
  const u32 xa0 = lds0 + (wm * 64 + l15) * 128 + ((g ^ (lane & 7)) * 16);
  const u32 xa1 = xa0 ^ 64;
  u32 wa[2][2];
  {
    const int q = l15 >> 2, p = lane & 3;
    UNR for (int nt = 0; nt < 2; ++nt) UNR for (int t = 0; t < 2; ++t) {
      const int swz = (q << 2) | ((2 * g + t) & 3);
      wa[nt][t] = lds0 + 16384 + 256 * (8 * g + 4 * t + q) + 16 * ((2 * (wn * 2 + nt) + (p >> 1)) ^ swz) + 8 * (p & 1);
    }
  }
  f32x4 acc[4][2];
  UNR for (int i = 0; i < 4; ++i) UNR for (int j = 0; j < 2; ++j) acc[i][j] = f32x4{0.f, 0.f, 0.f, 0.f};
#define K1_COMPUTE(kt) do { const u32 sb = ((kt) & 3) * K1_STAGE; \
    UNR for (int ks = 0; ks < 2; ++ks) { \
      u32x4 xf[4], wfr[2]; \
      UNR for (int nt = 0; nt < 2; ++nt) { u32x2 lo = lds_tr(wa[nt][0] + sb + ks * 8192), hi = lds_tr(wa[nt][1] + sb + ks * 8192); \
        wfr[nt] = u32x4{lo[0], lo[1], hi[0], hi[1]}; } \
      UNR for (int mt = 0; mt < 4; ++mt) xf[mt] = lds_r128((ks ? xa1 : xa0) + sb + mt * 2048); \
      UNR for (int mt = 0; mt < 4; ++mt) UNR for (int nt = 0; nt < 2; ++nt) \
        acc[mt][nt] = SWAP ? mfma16(wfr[nt], xf[mt], acc[mt][nt]) : mfma16(xf[mt], wfr[nt], acc[mt][nt]); \
    } } while (0)
  WAIT_BAR(28); K1_ISSUE(3); K1_COMPUTE(0);
  WAIT_BAR(28); K1_ISSUE(4); K1_COMPUTE(1);
  WAIT_BAR(28); K1_ISSUE(5); K1_COMPUTE(2);
  WAIT_BAR(8);
  asm volatile("" : "+v"(kk[0]), "+v"(kk[1]), "+v"(kk[2]), "+v"(kk[3]), "+v"(kk[4]), "+v"(kk[5]), "+v"(kk[6]),
                    "+v"(vv[0]), "+v"(vv[1]), "+v"(vv[2]), "+v"(vv[3]), "+v"(sh));
  asm volatile("" : "+v"(wf[0]), "+v"(wf[1]), "+v"(wf[2]), "+v"(wf[3]), "+v"(wf[4]), "+v"(wf[5]), "+v"(wf[6]), "+v"(wf[7]));
  K1_ISSUE(6);
  {
    if (s0 == 0) { kk[0] = 0; kk[1] = 0; kk[2] = 0; }
    u32x2 o0, o1; kv_compute(kk, vv, sh, o0, o1);
    const u32 a = lds0 + K1_KV + (2 * cp) * 144 + (60 - 4 * rg) * 2;
    lds_w64(a, o0); lds_w64(a + 144, o1);
  }
  K1_COMPUTE(3);
  WAIT_BAR(8); K1_ISSUE(7);
  {
    const u32x4 t = lds_r128(lds0 + K1_KV + (tid >> 3) * 144 + (tid & 7) * 16);
    char* dst = ws + WS_XR + (size_t)(cg * 64 + (tid >> 3)) * 4096 + (1984 - 64 * st) * 2 + (tid & 7) * 16;
    asm volatile("s_waitcnt lgkmcnt(0)\n\tglobal_store_dwordx4 %0, %1, off\n\ts_nop 1" :: "v"(dst), "v"(t) : "memory");
    const u32x4 w = u32x4{wf[0] | (wf[1] << 16), wf[2] | (wf[3] << 16), wf[4] | (wf[5] << 16), wf[6] | (wf[7] << 16)};
    char* wdst = ws + WS_WO + (size_t)(wofrag < 800 ? wofrag : 800) * 1024 + lane * 16;
    asm volatile("global_store_dwordx4 %0, %1, off\n\ts_nop 1" :: "v"(wdst), "v"(w) : "memory");
  }
  K1_COMPUTE(4);
  WAIT_BAR(10); K1_ISSUE(8); K1_COMPUTE(5);
  WAIT_BAR(10); K1_ISSUE(9); K1_COMPUTE(6);
  WAIT_BAR(8); K1_COMPUTE(7);
  WAIT_BAR(4); K1_COMPUTE(8);
  WAIT_BAR(0); K1_COMPUTE(9);
  if (SWAP) {
    const float sc = ct < 5 ? QSCALE : 1.0f;
    char* base = ws + (ct < 5 ? WS_Q : WS_K);
    UNR for (int nt = 0; nt < 2; ++nt) {
      const int c4 = ((ct % 5) * 128 + wn * 32 + nt * 16) / 4 + g;
      const int h = c4 / 5, ch = c4 - h * 5;
      UNR for (int mt = 0; mt < 4; ++mt) {
        const int tile = rt * 8 + wm * 4 + mt;
        const f32x4 a = acc[mt][nt];
        *(u32x2*)(base + ((size_t)(h * 128 + tile) * 5 + ch) * 128 + l15 * 8) = u32x2{pack2(a[0] * sc, a[1] * sc), pack2(a[2] * sc, a[3] * sc)};
      }
    }
  } else {
    UNR for (int nt = 0; nt < 2; ++nt) {
      const int c = (ct - 10) * 128 + wn * 32 + nt * 16 + l15;
      const int h = c / 20, d = c - h * 20;
      const int off = d < 16 ? g * 256 + d * 16 : 1024 + g * 64 + (d - 16) * 16;
      UNR for (int mp = 0; mp < 2; ++mp) {
        const int blk = rt * 4 + wm * 2 + mp;
        const f32x4 a = acc[2 * mp][nt], b = acc[2 * mp + 1][nt];
        *(u32x4*)(ws + WS_V + (size_t)(h * 64 + blk) * 1280 + off) =
            u32x4{pack2(a[0], a[1]), pack2(a[2], a[3]), pack2(b[0], b[1]), pack2(b[2], b[3])};
      }
    }
  }
}

__global__ __launch_bounds__(512, 2) void k1_kernel(const u16* __restrict__ x, const u16* __restrict__ wqkv, const u16* __restrict__ kin,
                                                     const u16* __restrict__ vin, const u16* __restrict__ shk, const u16* __restrict__ wo,
                                                     char* __restrict__ ws) {
  __shared__ __attribute__((aligned(1024))) char lds[K1_LDS];
  const u32 lds0 = (u32)(uintptr_t)lds;
  const int b = blockIdx.x, xcd = b & 7, idx = b >> 3, jr = xcd >> 1, jc = xcd & 1;
  const int tid = threadIdx.x, wid = __builtin_amdgcn_readfirstlane(tid >> 6);
  if (jc == 1 && idx >= 28) {
    const int hid = jr * 4 + (idx - 28);
    const int cp = tid & 31, rg = tid >> 5;
    for (int i = 0; i < 5; ++i) {
      const int task = 240 + hid * 5 + i, st = task / 10, cg = task % 10;
      const int s0 = st * 64 + rg * 4, c0 = cg * 64 + cp * 2;
      u32 kk[7], vv[4];
      UNR for (int j = 0; j < 7; ++j) kk[j] = *(const u32*)(kin + (size_t)(s0 - 3 + j) * 640 + c0);
      UNR for (int j = 0; j < 4; ++j) vv[j] = *(const u32*)(vin + (size_t)(s0 + j) * 640 + c0);
      const u32x4 sh = *(const u32x4*)(shk + c0 * 4);
      u32x2 o0, o1; kv_compute(kk, vv, sh, o0, o1);
      const u32 a = lds0 + i * 16384 + (2 * cp) * 144 + (60 - 4 * rg) * 2;
      lds_w64(a, o0); lds_w64(a + 144, o1);
    }
    __syncthreads();
    for (int i = 0; i < 5; ++i) {
      const int task = 240 + hid * 5 + i, st = task / 10, cg = task % 10;
      const u32x4 t = lds_r128(lds0 + i * 16384 + (tid >> 3) * 144 + (tid & 7) * 16);
      *(u32x4*)(ws + WS_XR + (size_t)(cg * 64 + (tid >> 3)) * 4096 + (1984 - 64 * st) * 2 + (tid & 7) * 16) = t;
    }
    return;
  }
  const int rt = jr * 4 + (idx & 3), ct = jc * 8 + (idx >> 2);
  const int task = jc == 0 ? jr * 32 + idx : 128 + jr * 28 + idx;
  const int wofrag = wid * 240 + task;
  if (ct < 10) k1_gemm<true>(x, wqkv, kin, vin, shk, wo, ws, lds0, rt, ct, task, wofrag);
  else k1_gemm<false>(x, wqkv, kin, vin, shk, wo, ws, lds0, rt, ct, task, wofrag);
}

constexpr int L2_K = 0, L2_V = 20480, L2_ONES = 40960, L2_FLAG = 40992, L2_CONV = 41216;
constexpr int HC_STRIDE = 4160, CONV_X = 4 * HC_STRIDE, CONV_CH = CONV_X + 4608;
constexpr int L2_RED = L2_CONV + 3 * CONV_CH;
constexpr int K2_LDS = L2_RED + 2 * 8192 + 15 * 1024;

DI float xmax4(float m) {
  auto r = __builtin_amdgcn_permlane32_swap(__float_as_uint(m), __float_as_uint(m), false, false);
  m = fmaxf(__uint_as_float(r[0]), __uint_as_float(r[1]));
  auto s = __builtin_amdgcn_permlane16_swap(__float_as_uint(m), __float_as_uint(m), false, false);
  return fmaxf(__uint_as_float(s[0]), __uint_as_float(s[1]));
}
DI float ex2(float v) { return __builtin_amdgcn_exp2f(v); }

DI void attn_pass(u32 ka, u32 kb, u32 va0, u32 va1, u32 v1inc, u32x4 qA, u32x4 qB, float nmA, float nmB, int kb0, int l15, int g,
                  f32x4& oA0, f32x4& oA1, f32x4& oB0, f32x4& oB1) {
  const f32x4 cA = f32x4{nmA, nmA, nmA, nmA}, cB = f32x4{nmB, nmB, nmB, nmB};
  oA0 = oA1 = oB0 = oB1 = f32x4{0.f, 0.f, 0.f, 0.f};
  UNR for (int kbk = 0; kbk < 9; ++kbk) {
    if (kbk >= kb0) {
      const u32x2 k0a = lds_r64(ka + kbk * 1280), k0b = lds_r64(kb + kbk * 1280);
      const u32x2 k1a = lds_r64(ka + kbk * 1280 + 640), k1b = lds_r64(kb + kbk * 1280 + 640);
      const u32x4 kf0 = u32x4{k0a[0], k0a[1], k0b[0], k0b[1]}, kf1 = u32x4{k1a[0], k1a[1], k1b[0], k1b[1]};
      const u32x4 vf0 = lds_r128(va0 + kbk * 1280), vf1 = lds_r128(va1 + kbk * v1inc);
      float pa[8], pb[8];
      {
        f32x4 s = mfma16(kf0, qA, cA);
        UNR for (int r = 0; r < 4; ++r) {
          float p = ex2(s[r]);
          if (kbk == 0) p = (4 * g + r > l15) ? p : 0.f;
          if (kbk == 8) p = (4 * g + r <= l15) ? p : 0.f;
          pa[r] = p;
        }
      }
      if (kbk < 8) { f32x4 s = mfma16(kf1, qA, cA); UNR for (int r = 0; r < 4; ++r) pa[4 + r] = ex2(s[r]); }
      else { UNR for (int r = 0; r < 4; ++r) pa[4 + r] = 0.f; }
      if (kbk > 0) { f32x4 s = mfma16(kf0, qB, cB); UNR for (int r = 0; r < 4; ++r) pb[r] = ex2(s[r]); }
      else { UNR for (int r = 0; r < 4; ++r) pb[r] = 0.f; }
      {
        f32x4 s = mfma16(kf1, qB, cB);
        UNR for (int r = 0; r < 4; ++r) {
          float p = ex2(s[r]);
          if (kbk == 0) p = (4 * g + r > l15) ? p : 0.f;
          if (kbk == 8) p = (4 * g + r <= l15) ? p : 0.f;
          pb[4 + r] = p;
        }
      }
      const u32x4 pA = u32x4{pack2(pa[0], pa[1]), pack2(pa[2], pa[3]), pack2(pa[4], pa[5]), pack2(pa[6], pa[7])};
      const u32x4 pB = u32x4{pack2(pb[0], pb[1]), pack2(pb[2], pb[3]), pack2(pb[4], pb[5]), pack2(pb[6], pb[7])};
      oA0 = mfma16(vf0, pA, oA0); oA1 = mfma16(vf1, pA, oA1);
      oB0 = mfma16(vf0, pB, oB0); oB1 = mfma16(vf1, pB, oB1);
    }
  }
}

template <int TMASK>
DI void conv_accum(u32 ha, u32 xa, int r0, int nr, f32x4 (&acc)[8]) {
  constexpr int NF = (TMASK & 0xC0) ? 8 : 6;
  for (int rr = r0; rr < r0 + nr; ++rr) {
    const u32 h0 = ha + rr * 64, x0 = xa + rr * 64;
    u32x4 A[8], B[8];
    UNR for (int i = 0; i < NF; ++i) {
      const u32x2 lo = lds_r64(h0 + i * 512), hi = lds_r64(h0 + i * 512 + 8);
      A[i] = u32x4{lo[0], lo[1], hi[0], hi[1]};
      B[i] = lds_r128(x0 + (7 - i) * 512);
    }
    UNR for (int L = 0; L < NF; ++L) {
      UNR for (int o = 0; o <= L; ++o) {
        if (L + o < 8 && ((TMASK >> (L + o)) & 1)) {
          acc[L + o] = mfma16(A[L], B[o], acc[L + o]);
          if (o != L) acc[L + o] = mfma16(A[o], B[L], acc[L + o]);
        }
      }
    }
  }
}

DI void conv_store(f32x4 a, int n, u32 xbase, float dcoef, char* y2row, int l15, int g) {
  const int s = 256 * n + 16 * l15 + 4 * g;
  const u32x2 xv = lds_r64(xbase + (2044 - s) * 2);
  const u32 y01 = pack2(a[0], a[1]), y23 = pack2(a[2], a[3]);
  const u32 d01 = pack2(bf_hi(xv[1]) * dcoef, bf_lo(xv[1]) * dcoef), d23 = pack2(bf_hi(xv[0]) * dcoef, bf_lo(xv[0]) * dcoef);
  *(u32x2*)(y2row + s * 2) = u32x2{pack2(bf_lo(y01) + bf_lo(d01), bf_hi(y01) + bf_hi(d01)), pack2(bf_lo(y23) + bf_lo(d23), bf_hi(y23) + bf_hi(d23))};
}
DI void lds_signal(u32 flag) {
  asm volatile("s_waitcnt lgkmcnt(0)" ::: "memory");
  __hip_atomic_fetch_add((LDS3 u32*)(uintptr_t)flag, 1u, __ATOMIC_RELAXED, __HIP_MEMORY_SCOPE_WORKGROUP);
}
DI void lds_wait(u32 flag, u32 n) {
  while (__hip_atomic_load((LDS3 u32*)(uintptr_t)flag, __ATOMIC_RELAXED, __HIP_MEMORY_SCOPE_WORKGROUP) < n) __builtin_amdgcn_s_sleep(1);
  asm volatile("" ::: "memory");
}

__global__ __launch_bounds__(1024, 4) void k2_kernel(const u16* __restrict__ ssmk, const u16* __restrict__ ssmD, char* __restrict__ ws) {
  __shared__ __attribute__((aligned(1024))) char lds[K2_LDS];
  const u32 lds0 = (u32)(uintptr_t)lds;
  const int tid = threadIdx.x, lane = tid & 63, wid = __builtin_amdgcn_readfirstlane(tid >> 6);
  const int l15 = lane & 15, g = lane >> 4;
  const int b = blockIdx.x, xcd = b & 7, idx = b >> 3;
  const int head = xcd + 8 * (idx >> 3), qb = idx & 7;
  const int cbase = 80 * xcd + 5 * (idx >> 1), odd = idx & 1;
  const int ch0 = cbase + (odd ? 3 : 0), ch2 = cbase + 2;
  {
    const char* kg = ws + WS_K + ((size_t)head * 128 + (qb * 16 - 16)) * 640 + lane * 16;
    const char* vg = ws + WS_V + ((size_t)head * 64 + (qb * 8 - 8)) * 1280 + lane * 16;
    for (int i = wid; i < 40; i += 16) {
      const int j = i < 20 ? i : i - 20;
      if (qb > 0 || j >= 10) glds16((i < 20 ? kg : vg) + j * 1024, lds0 + i * 1024);
    }
    if (wid >= 4) {
      const int j = wid - 4, c = j >> 2, p = j & 3;
      const int chn = c < 2 ? ch0 + c : ch2;
      glds16(ws + WS_XR + (size_t)chn * 4096 + p * 1024 + lane * 16, lds0 + L2_CONV + c * CONV_CH + CONV_X + p * 1024);
    }
    if (tid < 768) {
      const int c = tid >> 8, t = tid & 255;
      const int chn = c < 2 ? ch0 + c : ch2;
      const u16* hp = ssmk + (size_t)chn * 2048 + t * 8;
      const u32x4 d = *(const u32x4*)hp;
      u32x2 e = u32x2{0u, 0u};
      if (t < 255) e = *(const u32x2*)(hp + 8);
      const u32 a01 = __builtin_amdgcn_alignbit(d[1], d[0], 16), a12 = __builtin_amdgcn_alignbit(d[2], d[1], 16),
                a23 = __builtin_amdgcn_alignbit(d[3], d[2], 16), a34 = __builtin_amdgcn_alignbit(e[0], d[3], 16),
                a45 = __builtin_amdgcn_alignbit(e[1], e[0], 16);
      const u32 a = lds0 + L2_CONV + c * CONV_CH + 32 + t * 16;
      lds_w128(a, d);
      lds_w128(a + HC_STRIDE, u32x4{a01, a12, a23, a34});
      lds_w128(a + 2 * HC_STRIDE, u32x4{d[1], d[2], d[3], e[0]});
      lds_w128(a + 3 * HC_STRIDE, u32x4{a12, a23, a34, a45});
      if (t == 0) {
        const u32 z = 0u;
        lds_w128(a - 32, u32x4{z, z, z, z}); lds_w128(a - 16, u32x4{z, z, z, z});
        lds_w128(a - 32 + HC_STRIDE, u32x4{z, z, z, z}); lds_w128(a - 16 + HC_STRIDE, u32x4{z, z, z, d[0] << 16});
        lds_w128(a - 32 + 2 * HC_STRIDE, u32x4{z, z, z, z}); lds_w128(a - 16 + 2 * HC_STRIDE, u32x4{z, z, z, d[0]});
        lds_w128(a - 32 + 3 * HC_STRIDE, u32x4{z, z, z, z}); lds_w128(a - 16 + 3 * HC_STRIDE, u32x4{z, z, d[0] << 16, a01});
      }
    } else {
      const int t = tid - 768;
      if (t < 96) { const int c = t >> 5; lds_w128(lds0 + L2_CONV + c * CONV_CH + CONV_X + 4096 + (t & 31) * 16, u32x4{0u, 0u, 0u, 0u}); }
      else if (t == 96) lds_w128(lds0 + L2_ONES, u32x4{0x3f803f80u, 0x3f803f80u, 0x3f803f80u, 0x3f803f80u});
      else if (t == 97) lds_w128(lds0 + L2_FLAG, u32x4{0u, 0u, 0u, 0u});
    }
  }
  if (wid < 8) {
    const int qt = qb * 16 + wid * 2;
    const char* qg = ws + WS_Q + ((size_t)head * 128 + qt) * 640 + l15 * 8;
    const u32x2 qa0 = *(const u32x2*)(qg + g * 128), qb0 = *(const u32x2*)(qg + 640 + g * 128);
    u32x2 qa1 = *(const u32x2*)(qg + 512), qb1 = *(const u32x2*)(qg + 640 + 512);
    if (g != 0) { qa1 = u32x2{0u, 0u}; qb1 = u32x2{0u, 0u}; }
    const u32x4 qA = u32x4{qa0[0], qa0[1], qa1[0], qa1[1]}, qB = u32x4{qb0[0], qb0[1], qb1[0], qb1[1]};
    WAIT_BAR(0);
    const u32 ka = lds0 + L2_K + wid * 1280 + g * 128 + l15 * 8, kb = lds0 + L2_K + wid * 1280 + 512 + l15 * 8;
    const u32 va0 = lds0 + L2_V + wid * 1280 + g * 256 + l15 * 16;
    const u32 va1 = l15 < 4 ? lds0 + L2_V + wid * 1280 + 1024 + g * 64 + l15 * 16 : lds0 + L2_ONES;
    const u32 v1inc = l15 < 4 ? 1280u : 0u;
    const int kb0 = qb == 0 ? 8 - wid : 0;
    float mA, mB;
    {
      const u32x2 k0a = lds_r64(ka + 8 * 1280), k0b = lds_r64(kb + 8 * 1280), k1a = lds_r64(ka + 8 * 1280 + 640), k1b = lds_r64(kb + 8 * 1280 + 640);
      const f32x4 z = f32x4{0.f, 0.f, 0.f, 0.f};
      const f32x4 sA = mfma16(u32x4{k0a[0], k0a[1], k0b[0], k0b[1]}, qA, z), sB = mfma16(u32x4{k1a[0], k1a[1], k1b[0], k1b[1]}, qB, z);
      mA = -INFINITY; mB = -INFINITY;
      UNR for (int r = 0; r < 4; ++r) {
        const bool ok = 4 * g + r <= l15;
        mA = fmaxf(mA, ok ? sA[r] : -INFINITY); mB = fmaxf(mB, ok ? sB[r] : -INFINITY);
      }
      mA = xmax4(mA); mB = xmax4(mB);
    }
    f32x4 oA0, oA1, oB0, oB1;
    float lA, lB;
    for (int attempt = 0;; ++attempt) {
      attn_pass(ka, kb, va0, va1, v1inc, qA, qB, -mA, -mB, kb0, l15, g, oA0, oA1, oB0, oB1);
      lA = __shfl(oA1[0], 16 + l15); lB = __shfl(oB1[0], 16 + l15);
      const bool bad = !(lA <= 16777216.f) || !(lB <= 16777216.f);
      if (attempt == 1 || !__any(bad)) break;
      float tA = -INFINITY, tB = -INFINITY;
      const f32x4 z = f32x4{0.f, 0.f, 0.f, 0.f};
      for (int t = 2 * kb0; t < 18; ++t) {
        const u32x2 k0a = lds_r64(ka + t * 640), k0b = lds_r64(kb + t * 640);
        const u32x4 kf = u32x4{k0a[0], k0a[1], k0b[0], k0b[1]};
        const f32x4 sA = mfma16(kf, qA, z), sB = mfma16(kf, qB, z);
        UNR for (int r = 0; r < 4; ++r) {
          const int m = 4 * g + r;
          const bool okA = t == 0 ? (m > l15) : (t == 16 ? (m <= l15) : t < 16);
          const bool okB = t == 1 ? (m > l15) : (t == 17 ? (m <= l15) : t > 1);
          tA = fmaxf(tA, okA ? sA[r] : -INFINITY); tB = fmaxf(tB, okB ? sB[r] : -INFINITY);
        }
      }
      mA = xmax4(tA); mB = xmax4(tB);
    }
    const float iA = 1.0f / lA, iB = 1.0f / lB;
    {
      const int k4 = 5 * head + g, k5 = 5 * head + 4;
      char* oa = ws + WS_O + (size_t)qt * 20480 + l15 * 16;
      const int o4 = (k4 >> 3) * 1024 + ((k4 & 7) >> 1) * 256 + (k4 & 1) * 8, o5 = (k5 >> 3) * 1024 + ((k5 & 7) >> 1) * 256 + (k5 & 1) * 8;
      *(u32x2*)(oa + o4) = u32x2{pack2(oA0[0] * iA, oA0[1] * iA), pack2(oA0[2] * iA, oA0[3] * iA)};
      *(u32x2*)(oa + 20480 + o4) = u32x2{pack2(oB0[0] * iB, oB0[1] * iB), pack2(oB0[2] * iB, oB0[3] * iB)};
      if (g == 0) {
        *(u32x2*)(oa + o5) = u32x2{pack2(oA1[0] * iA, oA1[1] * iA), pack2(oA1[2] * iA, oA1[3] * iA)};
        *(u32x2*)(oa + 20480 + o5) = u32x2{pack2(oB1[0] * iB, oB1[1] * iB), pack2(oB1[2] * iB, oB1[3] * iB)};
      }
    }
  } else {
    const int cw = wid - 8;
    const int c = cw < 4 ? (cw >> 1) : 2;
    const int chn = c < 2 ? ch0 + c : ch2;
    const float dcoef = bf_lo((u32)ssmD[chn]);
    WAIT_BAR(0);
    const u32 cb = lds0 + L2_CONV + c * CONV_CH;
    const u32 ha = cb + ((l15 + 1) & 3) * HC_STRIDE + (8 * g + ((l15 + 1) & ~3)) * 2;
    const u32 xa = cb + CONV_X + 480 - 32 * l15 + 16 * g;
    char* y2row = ws + WS_Y2 + (size_t)chn * 4096;
    f32x4 acc[8];
    UNR for (int i = 0; i < 8; ++i) acc[i] = f32x4{0.f, 0.f, 0.f, 0.f};
    if (cw < 4) {
      const int hf = cw & 1;
      conv_accum<0xFF>(ha, xa, hf * 4, 4, acc);
      const u32 red = lds0 + L2_RED + c * 8192 + lane * 16;
      const u32 flag = lds0 + L2_FLAG + c * 4;
      if (hf == 0) { UNR for (int i = 0; i < 4; ++i) lds_w128(red + i * 1024, __builtin_bit_cast(u32x4, acc[4 + i])); }
      else { UNR for (int i = 0; i < 4; ++i) lds_w128(red + 4096 + i * 1024, __builtin_bit_cast(u32x4, acc[i])); }
      if (lane == 0) lds_signal(flag);
      lds_wait(flag, 2);
      if (hf == 0) { UNR for (int i = 0; i < 4; ++i) conv_store(acc[i] + __builtin_bit_cast(f32x4, lds_r128(red + 4096 + i * 1024)), i, cb + CONV_X, dcoef, y2row, l15, g); }
      else { UNR for (int i = 0; i < 4; ++i) conv_store(acc[4 + i] + __builtin_bit_cast(f32x4, lds_r128(red + i * 1024)), 4 + i, cb + CONV_X, dcoef, y2row, l15, g); }
    } else {
      const int qr = cw - 4;
      const u32 red = lds0 + L2_RED + 16384 + lane * 16;
      const u32 flag = lds0 + L2_FLAG + 8;
      if (!odd) {
        conv_accum<0xC4>(ha, xa, qr * 2, 2, acc);
        const int tl[3] = {7, 6, 2};
        UNR for (int j = 0; j < 3; ++j) if (qr != j) lds_w128(red + (j * 3 + (qr > j ? qr - 1 : qr)) * 1024, __builtin_bit_cast(u32x4, acc[tl[j]]));
        if (lane == 0) lds_signal(flag);
        lds_wait(flag, 4);
        UNR for (int j = 0; j < 3; ++j) if (qr == j) {
          f32x4 a = acc[tl[j]];
          UNR for (int w = 0; w < 3; ++w) a += __builtin_bit_cast(f32x4, lds_r128(red + (j * 3 + w) * 1024));
          conv_store(a, tl[j], cb + CONV_X, dcoef, y2row, l15, g);
        }
      } else {
        conv_accum<0x3B>(ha, xa, qr * 2, 2, acc);
        const int tl[5] = {5, 4, 3, 1, 0};
        UNR for (int j = 0; j < 5; ++j) { const int ow = j & 3; if (qr != ow) lds_w128(red + (j * 3 + (qr > ow ? qr - 1 : qr)) * 1024, __builtin_bit_cast(u32x4, acc[tl[j]])); }
        if (lane == 0) lds_signal(flag);
        lds_wait(flag, 4);
        UNR for (int j = 0; j < 5; ++j) if (qr == (j & 3)) {
          f32x4 a = acc[tl[j]];
          UNR for (int w = 0; w < 3; ++w) a += __builtin_bit_cast(f32x4, lds_r128(red + (j * 3 + w) * 1024));
          conv_store(a, tl[j], cb + CONV_X, dcoef, y2row, l15, g);
        }
      }
    }
  }
}

constexpr int K3_LDS = 60 * 1024 + 80 * 144;

__global__ __launch_bounds__(512, 2) void k3_kernel(const u16* __restrict__ qin, const char* __restrict__ ws, u16* __restrict__ out) {
  __shared__ __attribute__((aligned(1024))) char lds[K3_LDS];
  const u32 lds0 = (u32)(uintptr_t)lds;
  const int tid = threadIdx.x, lane = tid & 63, wid = __builtin_amdgcn_readfirstlane(tid >> 6);
  const int l15 = lane & 15, g = lane >> 4;
  const int b = blockIdx.x, xcd = b & 7, idx = b >> 3;
  const int rb = xcd * 4 + (idx & 3), cb = idx >> 2;
  if (wid < 4) {
    const char* og = ws + WS_O + ((size_t)(rb * 4) * 20 + wid * 5) * 1024 + lane * 16;
    const char* wg = ws + WS_WO + ((size_t)(cb * 5) * 20 + wid * 5) * 1024 + lane * 16;
    f32x4 acc[4][5];
    UNR for (int i = 0; i < 4; ++i) UNR for (int j = 0; j < 5; ++j) acc[i][j] = f32x4{0.f, 0.f, 0.f, 0.f};
    UNR for (int ks = 0; ks < 5; ++ks) {
      u32x4 of[4], wf[5];
      UNR for (int i = 0; i < 4; ++i) of[i] = *(const u32x4*)(og + i * 20480 + ks * 1024);
      UNR for (int j = 0; j < 5; ++j) wf[j] = *(const u32x4*)(wg + j * 20480 + ks * 1024);
      UNR for (int i = 0; i < 4; ++i) UNR for (int j = 0; j < 5; ++j) acc[i][j] = mfma16(wf[j], of[i], acc[i][j]);
    }
    UNR for (int i = 0; i < 4; ++i) if (i != wid) {
      const int slot = i * 3 + (wid > i ? wid - 1 : wid);
      UNR for (int j = 0; j < 5; ++j) lds_w128(lds0 + (slot * 5 + j) * 1024 + lane * 16, __builtin_bit_cast(u32x4, acc[i][j]));
    }
    __syncthreads();
    UNR for (int i = 0; i < 4; ++i) if (i == wid) {
      u16* orow = out + (size_t)(rb * 64 + i * 16 + l15) * 1280 + 640 + cb * 80 + 4 * g;
      UNR for (int j = 0; j < 5; ++j) {
        f32x4 a = acc[i][j];
        UNR for (int w = 0; w < 3; ++w) a += __builtin_bit_cast(f32x4, lds_r128(lds0 + ((i * 3 + w) * 5 + j) * 1024 + lane * 16));
        *(u32x2*)(orow + j * 16) = u32x2{pack2(a[0], a[1]), pack2(a[2], a[3])};
      }
    }
  } else {
    const int t = tid - 256;
    const u32 yb = lds0 + 60 * 1024;
    u32x4 yv[3];
    UNR for (int i = 0; i < 3; ++i) { const int it = t + i * 256; if (it < 640) yv[i] = *(const u32x4*)(ws + WS_Y2 + (size_t)(cb * 80 + (it >> 3)) * 4096 + rb * 128 + (it & 7) * 16); }
    const int row = rb * 64 + (wid - 4) * 16 + l15;
    u32x4 qv[3];
    UNR for (int i = 0; i < 3; ++i) { const int ck = g + 4 * i; if (ck < 10) qv[i] = *(const u32x4*)(qin + (size_t)row * 640 + cb * 80 + ck * 8); }
    UNR for (int i = 0; i < 3; ++i) { const int it = t + i * 256; if (it < 640) lds_w128(yb + (it >> 3) * 144 + (it & 7) * 16, yv[i]); }
    __syncthreads();
    UNR for (int i = 0; i < 3; ++i) {
      const int ck = (g + 4 * i) < 10 ? (g + 4 * i) : 0;
      const int q = l15 >> 2, p = lane & 3;
      const u32 a = yb + (ck * 8 + q) * 144 + ((wid - 4) * 16 + 4 * p) * 2;
      u32x2 y0 = lds_tr(a), y1 = lds_tr(a + 4 * 144);
      asm volatile("" : "+v"(y0), "+v"(y1));
      if (g + 4 * i < 10) {
        const u32x4 qq = qv[i];
        const u32x4 r = u32x4{pack2(bf_lo(qq[0]) * bf_lo(y0[0]), bf_hi(qq[0]) * bf_hi(y0[0])), pack2(bf_lo(qq[1]) * bf_lo(y0[1]), bf_hi(qq[1]) * bf_hi(y0[1])),
                              pack2(bf_lo(qq[2]) * bf_lo(y1[0]), bf_hi(qq[2]) * bf_hi(y1[0])), pack2(bf_lo(qq[3]) * bf_lo(y1[1]), bf_hi(qq[3]) * bf_hi(y1[1]))};
        *(u32x4*)(out + (size_t)row * 1280 + cb * 80 + (g + 4 * i) * 8) = r;
      }
    }
  }
}

extern "C" void kernel_launch(void* const* d_in, const int* in_sizes, int n_in, void* d_out, int out_size, void* d_ws, size_t ws_size,
                              hipStream_t stream) {
  const u16* q = (const u16*)d_in[0];
  const u16* k = (const u16*)d_in[1];
  const u16* v = (const u16*)d_in[2];
  const u16* shk = (const u16*)d_in[3];
  const u16* ssmk = (const u16*)d_in[4];
  const u16* ssmD = (const u16*)d_in[5];
  const u16* x = (const u16*)d_in[6];
  const u16* wqkv = (const u16*)d_in[7];
  const u16* wo = (const u16*)d_in[8];
  char* ws = (char*)d_ws;
  k1_kernel<<<256, 512, 0, stream>>>(x, wqkv, k, v, shk, wo, ws);
  k2_kernel<<<256, 1024, 0, stream>>>(ssmk, ssmD, ws);
  k3_kernel<<<256, 512, 0, stream>>>(q, ws, (u16*)d_out);
}
```

```cpp
#include <hip/hip_runtime.h>
#include <hip/hip_bf16.h>
#include <stdint.h>

typedef unsigned short u16;
typedef unsigned int u32;
typedef unsigned long long u64;
typedef __bf16 bf16x8 __attribute__((ext_vector_type(8)));
typedef __bf16 bf16x2 __attribute__((ext_vector_type(2)));
typedef float f32x4 __attribute__((ext_vector_type(4)));
typedef float f32x2 __attribute__((ext_vector_type(2)));
typedef u32 u32x2 __attribute__((ext_vector_type(2)));
typedef u32 u32x4 __attribute__((ext_vector_type(4)));
typedef short s16x4 __attribute__((ext_vector_type(4)));
typedef __amdgpu_buffer_rsrc_t rsrc_t;
#define LDS3 __attribute__((address_space(3)))
#define DI __device__ __forceinline__
#define UNR _Pragma("unroll")
#define SBAR() __builtin_amdgcn_sched_barrier(0)

constexpr u32 PLANE = 2048u * 640u * 2u;
constexpr u32 WS_XR = 0;
constexpr u32 WS_Q = 1 * PLANE;
constexpr u32 WS_K = 2 * PLANE;
constexpr u32 WS_V = 3 * PLANE;
constexpr u32 WS_O = 4 * PLANE;
constexpr u32 WS_Y2 = 5 * PLANE;
constexpr u32 WS_EPOCH = 6 * PLANE;
#define FSTRIDE 128
constexpr u32 WS_F1 = WS_EPOCH + 2048, WS_F2 = WS_F1 + 256 * FSTRIDE, WS_F3 = WS_F2 + 256 * FSTRIDE, WS_F4 = WS_F3 + 256 * FSTRIDE;
constexpr u32 WS_END = WS_F4 + 256 * FSTRIDE;
constexpr float QSCALE = 0.22360679774997896f * 1.4426950408889634f;

constexpr u32 L_STAGE = 32768, L_KV = 65536, L_HC = 98304, HC_STRIDE = 4160, HC_CH = 16640, L_XR = 148224, XR_CH = 4608, L_CNT = 162048, LDS_BYTES = 162304;
constexpr u32 L_AK = 0, L_AV = 24576, L_ONES = 45056, L_RED1 = 49152, L_RED2 = 65536;
constexpr u32 L_TAG = L_CNT + 240;
constexpr u32 L_WO = 45312, L_GT = 148224, L_RED3 = 0;
enum { C_B = 0, C_QKV, C_KV, C_O, C_OOK, C_XR, C_P0, C_P1, C_Q4, C_Y, C_YOK, C_G, C_W, C_R0, C_R1, C_R2, C_R3, C_S1, C_S2, C_H, C_TAG,
       C_FULL = 24, C_EMPTY = 36, C_TAGV = 48, C_NUM = 52 };
constexpr u32 SPIN_CAP = 1u << 22;
#define POLL_SLEEP 4

#define STAMP_DECL
#define STAMP(i)
#define CSTAMP(i)
#define STAMP_FLUSH(wsp)

DI float bf_lo(u32 v) { return __uint_as_float(v << 16); }
DI float bf_hi(u32 v) { return __uint_as_float(v & 0xffff0000u); }
DI u32 pack2(float lo, float hi) { f32x2 v = {lo, hi}; return __builtin_bit_cast(u32, __builtin_convertvector(v, bf16x2)); }
DI f32x4 mfma16(u32x4 a, u32x4 b, f32x4 c) {
  return __builtin_amdgcn_mfma_f32_16x16x32_bf16(__builtin_bit_cast(bf16x8, a), __builtin_bit_cast(bf16x8, b), c, 0, 0, 0);
}
#define ST_AUX 16
#define LD_AUX 16
#define LD_POL "sc1"
DI void glds16(const void* g, u32 lds_base) {
  u32 sv;
  asm volatile("s_mov_b32 %0, m0\n\ts_mov_b32 m0, %2\n\ts_nop 0\n\tglobal_load_lds_dwordx4 %1, off\n\ts_mov_b32 m0, %0"
               : "=&s"(sv) : "v"(g), "s"(lds_base) : "memory");
}
DI void glds16_sc1(const void* g, u32 lds_base) {
  u32 sv;
  asm volatile("s_mov_b32 %0, m0\n\ts_mov_b32 m0, %2\n\ts_nop 0\n\tglobal_load_lds_dwordx4 %1, off " LD_POL "\n\ts_mov_b32 m0, %0"
               : "=&s"(sv) : "v"(g), "s"(lds_base) : "memory");
}
DI u32x4 lds_r128(u32 a) { return *(const LDS3 u32x4*)(uintptr_t)a; }
DI u32x2 lds_r64(u32 a) { return *(const LDS3 u32x2*)(uintptr_t)a; }
DI u32x2 lds_r64v(u32 a) { return *(const volatile LDS3 u32x2*)(uintptr_t)a; }
DI void lds_w128(u32 a, u32x4 v) { *(LDS3 u32x4*)(uintptr_t)a = v; }
DI u32x2 lds_tr(u32 a) { return __builtin_bit_cast(u32x2, __builtin_amdgcn_ds_read_tr16_b64_v4i16((LDS3 s16x4*)(uintptr_t)a)); }
DI void st128_sc1(rsrc_t r, u32 off, u32x4 v) { __builtin_amdgcn_raw_buffer_store_b128(v, r, off, 0, ST_AUX); }
DI void st64_sc1(rsrc_t r, u32 off, u32x2 v) { __builtin_amdgcn_raw_buffer_store_b64(v, r, off, 0, 16); }
DI u32x4 ld128_sc1(rsrc_t r, u32 off) { return __builtin_amdgcn_raw_buffer_load_b128(r, off, 0, LD_AUX); }
DI u32x2 ld64_sc1(rsrc_t r, u32 off) { return __builtin_amdgcn_raw_buffer_load_b64(r, off, 0, 16); }
#define VM_BAR(N) asm volatile("s_waitcnt vmcnt(" #N ") lgkmcnt(0)\n\ts_barrier" ::: "memory")
#define LG_BAR() asm volatile("s_waitcnt lgkmcnt(0)\n\ts_barrier" ::: "memory")
#define VM0() asm volatile("s_waitcnt vmcnt(0)" ::: "memory")

DI u32 cnt_add(u32 lds0, int c) {
  asm volatile("s_waitcnt lgkmcnt(0)" ::: "memory");
  u32 old = 0;
  if ((threadIdx.x & 63) == 0) old = __hip_atomic_fetch_add((LDS3 u32*)(uintptr_t)(lds0 + L_CNT + 4 * c), 1u, __ATOMIC_RELAXED, __HIP_MEMORY_SCOPE_WORKGROUP);
  return __builtin_amdgcn_readfirstlane(old);
}
DI void cnt_sig(u32 lds0, int c) {
  asm volatile("s_waitcnt lgkmcnt(0)" ::: "memory");
  if ((threadIdx.x & 63) == 0) (void)__hip_atomic_fetch_add((LDS3 u32*)(uintptr_t)(lds0 + L_CNT + 4 * c), 1u, __ATOMIC_RELAXED, __HIP_MEMORY_SCOPE_WORKGROUP);
  asm volatile("" ::: "memory");
}
DI void cnt_wait(u32 lds0, int c, u32 n) {
  _Pragma("unroll 1") for (u32 spins = 0; spins < SPIN_CAP; ++spins) {
    if (__builtin_amdgcn_readfirstlane(__hip_atomic_load((LDS3 u32*)(uintptr_t)(lds0 + L_CNT + 4 * c), __ATOMIC_RELAXED, __HIP_MEMORY_SCOPE_WORKGROUP)) >= n) break;
    __builtin_amdgcn_s_sleep(1);
  }
  asm volatile("" ::: "memory");
}
DI void flag_publish(rsrc_t r, u32 off, u32x2 tag) { if ((threadIdx.x & 63) == 0) st64_sc1(r, off, tag); }
DI bool flags_ok(u32x2 v, u32x2 tag, bool need) { return __all(!need || (v[0] == tag[0] && v[1] == tag[1])); }
DI void flags_wait(rsrc_t r, u32 off, bool need, u32x2 tag) {
  _Pragma("unroll 1") for (u32 spins = 0; spins < SPIN_CAP; ++spins) {
    asm volatile("" ::: "memory");
    const u32x2 v = ld64_sc1(r, off);
    if (flags_ok(v, tag, need)) break;
    __builtin_amdgcn_s_sleep(POLL_SLEEP);
  }
  asm volatile("" ::: "memory");
}
DI u32x4 swap_pack(u32 a0, u32 a1, u32 b0, u32 b1) {
  auto s0 = __builtin_amdgcn_permlane16_swap(a0, b0, false, false);
  auto s1 = __builtin_amdgcn_permlane16_swap(a1, b1, false, false);
  return u32x4{s0[0], s1[0], s0[1], s1[1]};
}
DI float xmax4(float m) {
  auto r = __builtin_amdgcn_permlane32_swap(__float_as_uint(m), __float_as_uint(m), false, false);
  m = fmaxf(__uint_as_float(r[0]), __uint_as_float(r[1]));
  auto s = __builtin_amdgcn_permlane16_swap(__float_as_uint(m), __float_as_uint(m), false, false);
  return fmaxf(__uint_as_float(s[0]), __uint_as_float(s[1]));
}
DI float ex2(float v) { return __builtin_amdgcn_exp2f(v); }

template <bool SWAP>
DI void gemm1_compute(u32 lds0, rsrc_t wr, int wid, int lane, int rt, int ct, u32 f1off, u32x2 tag) {
  asm volatile("" : "+v"(lane));
  const int l15 = lane & 15, g = lane >> 4, wm = wid >> 2, wn = wid & 3;
  const u32 xa0 = lds0 + (wm * 64 + l15) * 128 + ((g ^ (lane & 7)) * 16), xa1 = xa0 ^ 64;
  u32 wa[2][2];
  {
    const int q = l15 >> 2, p = lane & 3;
    UNR for (int nt = 0; nt < 2; ++nt) UNR for (int t = 0; t < 2; ++t) {
      const int swz = (q << 2) | ((2 * g + t) & 3);
      wa[nt][t] = lds0 + 16384 + 256 * (8 * g + 4 * t + q) + 16 * ((2 * (wn * 2 + nt) + (p >> 1)) ^ swz) + 8 * (p & 1);
    }
  }
  f32x4 acc[4][2];
  UNR for (int i = 0; i < 4; ++i) UNR for (int j = 0; j < 2; ++j) acc[i][j] = f32x4{0.f, 0.f, 0.f, 0.f};
  u32x4 xf[2][4], wf[2][2];
#define G1_RW(set, kt, ks, nt) do { const u32 sb_ = ((kt) % 3) * L_STAGE; \
    const u32x2 lo = lds_tr(wa[nt][0] + sb_ + (ks) * 8192), hi = lds_tr(wa[nt][1] + sb_ + (ks) * 8192); wf[set][nt] = u32x4{lo[0], lo[1], hi[0], hi[1]}; } while (0)
#define G1_RX(set, kt, ks, mt) xf[set][mt] = lds_r128(((ks) ? xa1 : xa0) + ((kt) % 3) * L_STAGE + (mt) * 2048)
#define G1_M(set, mt, nt) acc[mt][nt] = SWAP ? mfma16(wf[set][nt], xf[set][mt], acc[mt][nt]) : mfma16(xf[set][mt], wf[set][nt], acc[mt][nt])
#define G1_HALF(cs, ns, kt, ks, RD) do { \
    G1_M(cs, 0, 0); if (RD) G1_RW(ns, kt, ks, 0); SBAR(); \
    G1_M(cs, 0, 1); if (RD) { G1_RX(ns, kt, ks, 0); G1_RX(ns, kt, ks, 1); } SBAR(); \
    G1_M(cs, 1, 0); if (RD) G1_RW(ns, kt, ks, 1); SBAR(); \
    G1_M(cs, 1, 1); if (RD) G1_RX(ns, kt, ks, 2); SBAR(); \
    G1_M(cs, 2, 0); if (RD) G1_RX(ns, kt, ks, 3); SBAR(); \
    G1_M(cs, 2, 1); G1_M(cs, 3, 0); G1_M(cs, 3, 1); SBAR(); } while (0)
  G1_RW(0, 0, 0, 0); G1_RX(0, 0, 0, 0); G1_RW(0, 0, 0, 1); G1_RX(0, 0, 0, 1); G1_RX(0, 0, 0, 2); G1_RX(0, 0, 0, 3);
  SBAR();
  UNR for (int t = 0; t < 10; ++t) {
    G1_HALF(0, 1, t, 1, true);
    if (t < 9) {
      if (t == 2) VM_BAR(0); else LG_BAR();
      if (t == 2 && wid == 0) flag_publish(wr, f1off, tag);
      SBAR();
    }
    G1_HALF(1, 0, t + 1, 0, t < 9);
  }
  if (SWAP) {
    const float sc = ct < 5 ? QSCALE : 1.0f;
    const u32 base = ct < 5 ? WS_Q : WS_K;
    UNR for (int nt = 0; nt < 2; ++nt) {
      const int G8 = ((ct % 5) * 128 + wn * 32 + nt * 16) / 8 + (g >> 1);
      const int pr = G8 / 5, gr = G8 - pr * 5;
      UNR for (int mp = 0; mp < 2; ++mp) {
        const f32x4 a = acc[2 * mp][nt], b = acc[2 * mp + 1][nt];
        const u32x4 v = swap_pack(pack2(a[0] * sc, a[1] * sc), pack2(a[2] * sc, a[3] * sc), pack2(b[0] * sc, b[1] * sc), pack2(b[2] * sc, b[3] * sc));
        const int tile = rt * 8 + wm * 4 + 2 * mp + (g & 1);
        st128_sc1(wr, base + ((pr * 128 + tile) * 5 + gr) * 256 + l15 * 16, v);
      }
    }
  } else {
    UNR for (int nt = 0; nt < 2; ++nt) {
      const int c = (ct - 10) * 128 + wn * 32 + nt * 16 + l15;
      const int h = c / 20, d = c - h * 20;
      const int off = d < 16 ? g * 256 + d * 16 : 1024 + g * 64 + (d - 16) * 16;
      UNR for (int mp = 0; mp < 2; ++mp) {
        const int blk = rt * 4 + wm * 2 + mp;
        const f32x4 a = acc[2 * mp][nt], b = acc[2 * mp + 1][nt];
        st128_sc1(wr, WS_V + (h * 64 + blk) * 1280 + off, u32x4{pack2(a[0], a[1]), pack2(a[2], a[3]), pack2(b[0], b[1]), pack2(b[2], b[3])});
      }
    }
  }
}

struct AttnOut { f32x4 oA0, oA1, oB0, oB1; };

DI void attn_slow(u32 ka, u32 va0, u32 va1, u32 v1inc, u32x4 qA, u32x4 qB, int kb0, int l15, int g, AttnOut& o) {
  const f32x4 z = f32x4{0.f, 0.f, 0.f, 0.f};
  float tA = -INFINITY, tB = -INFINITY;
  for (int t = 2 * kb0; t < 18; ++t) {
    const u32x4 kf = lds_r128(ka + t * 768);
    const f32x4 sA = mfma16(kf, qA, z), sB = mfma16(kf, qB, z);
    UNR for (int r = 0; r < 4; ++r) {
      const int dA = 16 * t + 4 * g + r - l15;
      tA = fmaxf(tA, (dA >= 1 && dA <= 256) ? sA[r] : -INFINITY);
      tB = fmaxf(tB, (dA >= 17 && dA <= 272) ? sB[r] : -INFINITY);
    }
  }
  const float mA = xmax4(tA), mB = xmax4(tB);
  o.oA0 = o.oA1 = o.oB0 = o.oB1 = z;
  for (int kbk = kb0; kbk < 9; ++kbk) {
    float pa[8], pb[8];
    UNR for (int hf = 0; hf < 2; ++hf) {
      const int t = 2 * kbk + hf;
      const u32x4 kf = lds_r128(ka + t * 768);
      const f32x4 sA = mfma16(kf, qA, z), sB = mfma16(kf, qB, z);
      UNR for (int r = 0; r < 4; ++r) {
        const int dA = 16 * t + 4 * g + r - l15;
        pa[4 * hf + r] = (dA >= 1 && dA <= 256) ? ex2(sA[r] - mA) : 0.f;
        pb[4 * hf + r] = (dA >= 17 && dA <= 272) ? ex2(sB[r] - mB) : 0.f;
      }
    }
    const u32x4 vf0 = lds_r128(va0 + kbk * 1280), vf1 = lds_r128(va1 + kbk * v1inc);
    const u32x4 pA = u32x4{pack2(pa[0], pa[1]), pack2(pa[2], pa[3]), pack2(pa[4], pa[5]), pack2(pa[6], pa[7])};
    const u32x4 pB = u32x4{pack2(pb[0], pb[1]), pack2(pb[2], pb[3]), pack2(pb[4], pb[5]), pack2(pb[6], pb[7])};
    o.oA0 = mfma16(vf0, pA, o.oA0); o.oA1 = mfma16(vf1, pA, o.oA1);
    o.oB0 = mfma16(vf0, pB, o.oB0); o.oB1 = mfma16(vf1, pB, o.oB1);
  }
}

DI void attn_fast(u32 ka, u32 va0, u32 va1, u32 v1inc, u32x4 qA, u32x4 qB, int l15, int g, AttnOut& o) {
  const f32x4 z = f32x4{0.f, 0.f, 0.f, 0.f};
  bool lo[4];
  UNR for (int r = 0; r < 4; ++r) lo[r] = 4 * g + r <= l15;
  u32x4 kf[2][2], vf[2][2], pA, pB;
  f32x4 S[2][4];
  kf[1][0] = lds_r128(ka + 16 * 768); kf[1][1] = lds_r128(ka + 17 * 768);
  kf[0][0] = lds_r128(ka); kf[0][1] = lds_r128(ka + 768);
  vf[1][0] = lds_r128(va0 + 8 * 1280); vf[1][1] = lds_r128(va1 + 8 * v1inc);
  f32x4 cA, cB;
  {
    const f32x4 sA = mfma16(kf[1][0], qA, z), sB = mfma16(kf[1][1], qB, z), sB0 = mfma16(kf[1][0], qB, z);
    SBAR();
    kf[1][0] = lds_r128(ka + 2 * 768); kf[1][1] = lds_r128(ka + 3 * 768);
    float mA = -INFINITY, mB = -INFINITY;
    UNR for (int r = 0; r < 4; ++r) { mA = fmaxf(mA, lo[r] ? sA[r] : -INFINITY); mB = fmaxf(mB, lo[r] ? sB[r] : -INFINITY); }
    mA = xmax4(mA); mB = xmax4(mB);
    cA = f32x4{-mA, -mA, -mA, -mA}; cB = f32x4{-mB, -mB, -mB, -mB};
    S[0][0] = mfma16(kf[0][0], qA, cA); S[0][1] = mfma16(kf[0][1], qA, cA); S[0][3] = mfma16(kf[0][1], qB, cB);
    float a[4], b0[4], b1[4];
    UNR for (int r = 0; r < 4; ++r) { a[r] = lo[r] ? ex2(sA[r] - mA) : 0.f; b0[r] = ex2(sB0[r] - mB); b1[r] = lo[r] ? ex2(sB[r] - mB) : 0.f; }
    pA = u32x4{pack2(a[0], a[1]), pack2(a[2], a[3]), 0u, 0u};
    pB = u32x4{pack2(b0[0], b0[1]), pack2(b0[2], b0[3]), pack2(b1[0], b1[1]), pack2(b1[2], b1[3])};
  }
  o.oA0 = o.oA1 = o.oB0 = o.oB1 = z;
  SBAR();
  UNR for (int k = 0; k < 8; ++k) {
    const int c = k & 1, n = c ^ 1;
    vf[c][0] = lds_r128(va0 + k * 1280); vf[c][1] = lds_r128(va1 + k * v1inc);
    if (k < 6) { kf[c][0] = lds_r128(ka + (2 * k + 4) * 768); kf[c][1] = lds_r128(ka + (2 * k + 5) * 768); }
    SBAR();
    u32 np[8];
    UNR for (int i = 0; i < 4; ++i) {
      if (k < 7) S[n][i] = mfma16(kf[n][i & 1], i < 2 ? qA : qB, i < 2 ? cA : cB);
      float e[4];
      if (k == 0 && i == 2) { np[4] = 0u; np[5] = 0u; }
      else {
        e[0] = ex2(S[c][i][0]); e[1] = ex2(S[c][i][1]);
      }
      if (i == 0) o.oA0 = mfma16(vf[n][0], pA, o.oA0);
      if (i == 1) o.oA1 = mfma16(vf[n][1], pA, o.oA1);
      if (i == 2) o.oB0 = mfma16(vf[n][0], pB, o.oB0);
      if (i == 3) o.oB1 = mfma16(vf[n][1], pB, o.oB1);
      if (!(k == 0 && i == 2)) {
        e[2] = ex2(S[c][i][2]); e[3] = ex2(S[c][i][3]);
        if (k == 0 && (i == 0 || i == 3)) { UNR for (int r = 0; r < 4; ++r) e[r] = lo[r] ? 0.f : e[r]; }
        np[2 * i] = pack2(e[0], e[1]); np[2 * i + 1] = pack2(e[2], e[3]);
      }
      SBAR();
    }
    pA = u32x4{np[0], np[1], np[2], np[3]}; pB = u32x4{np[4], np[5], np[6], np[7]};
  }
  o.oA0 = mfma16(vf[1][0], pA, o.oA0); o.oA1 = mfma16(vf[1][1], pA, o.oA1);
  o.oB0 = mfma16(vf[1][0], pB, o.oB0); o.oB1 = mfma16(vf[1][1], pB, o.oB1);
}

template <int TMASK, int NR>
DI void conv_accum(u32 ha, u32 xa, f32x4 (&acc)[8]) {
  constexpr int NF = (TMASK & 0xC0) ? 8 : 6;
  u32x4 A[8], B[8];
#define CV_LA(i, rr) do { if ((i) < NF) { const u32x2 lo_ = lds_r64v(ha + (rr) * 64 + (i) * 512), hi_ = lds_r64v(ha + (rr) * 64 + (i) * 512 + 8); \
    A[i] = u32x4{lo_[0], lo_[1], hi_[0], hi_[1]}; } } while (0)
#define CV_LB(m, rr) do { if ((m) < NF) B[m] = lds_r128(xa + (rr) * 64 + (7 - (m)) * 512); } while (0)
#define CV_MM(i, m) do { if ((i) < NF && (m) < NF && (i) + (m) < 8 && ((TMASK >> ((i) + (m))) & 1)) acc[(i) + (m)] = mfma16(A[i], B[m], acc[(i) + (m)]); } while (0)
  CV_LB(0, 0); UNR for (int i = 7; i >= 4; --i) CV_LA(i, 0);
  UNR for (int m = 1; m < 4; ++m) CV_LB(m, 0);
  UNR for (int i = 0; i < 4; ++i) { CV_LA(i, 0); CV_LB(7 - i, 0); }
  SBAR();
  UNR for (int rr = 0; rr < NR; ++rr) {
    UNR for (int m = 0; m < 4; ++m) UNR for (int i = 7; i >= 4; --i) CV_MM(i, m);
    SBAR();
    if (rr + 1 < NR) { UNR for (int i = 7; i >= 4; --i) CV_LA(i, rr + 1); }
    SBAR();
    UNR for (int i = 0; i < 4; ++i) UNR for (int m = 7; m >= 4; --m) CV_MM(i, m);
    SBAR();
    if (rr + 1 < NR) { UNR for (int m = 7; m >= 4; --m) CV_LB(m, rr + 1); }
    SBAR();
    UNR for (int m = 0; m < 4; ++m) {
      UNR for (int i = 0; i < 4; ++i) CV_MM(i, m);
      SBAR();
      if (rr + 1 < NR) CV_LB(m, rr + 1);
      SBAR();
    }
    if (rr + 1 < NR) { UNR for (int i = 0; i < 4; ++i) CV_LA(i, rr + 1); }
    SBAR();
  }
}
DI u32x2 conv_fin(f32x4 a, int n, u32 xbase, float dcoef, int l15, int g) {
  const int s = 256 * n + 16 * l15 + 4 * g;
  const u32x2 xv = lds_r64(xbase + (2044 - s) * 2);
  const u32 y01 = pack2(a[0], a[1]), y23 = pack2(a[2], a[3]);
  const u32 d01 = pack2(bf_hi(xv[1]) * dcoef, bf_lo(xv[1]) * dcoef), d23 = pack2(bf_hi(xv[0]) * dcoef, bf_lo(xv[0]) * dcoef);
  return u32x2{pack2(bf_lo(y01) + bf_lo(d01), bf_hi(y01) + bf_hi(d01)), pack2(bf_lo(y23) + bf_lo(d23), bf_hi(y23) + bf_hi(d23))};
}
DI void conv_store2(rsrc_t wr, u32 rowoff, u32x2 ya, int na, u32x2 yb, int nb, int l15, int g) {
  const u32x4 v = swap_pack(ya[0], ya[1], yb[0], yb[1]);
  st128_sc1(wr, rowoff + (256 * ((g & 1) ? nb : na) + 16 * l15 + 8 * (g >> 1)) * 2, v);
}
DI void conv_store1(rsrc_t wr, u32 rowoff, u32x2 ya, int na, int l15, int g) { st64_sc1(wr, rowoff + (256 * na + 16 * l15 + 4 * g) * 2, ya); }

DI u32 kv_pair(u32 k0, u32 k1, u32 k2, u32 k3, u32 v, u32 sa01, u32 sa23, u32 sb01, u32 sb23) {
  u32 p = pack2(bf_lo(k0) * bf_hi(sa23), bf_hi(k0) * bf_hi(sb23));
  u32 q = pack2(bf_lo(k1) * bf_lo(sa23), bf_hi(k1) * bf_lo(sb23));
  p = pack2(bf_lo(p) + bf_lo(q), bf_hi(p) + bf_hi(q));
  q = pack2(bf_lo(k2) * bf_hi(sa01), bf_hi(k2) * bf_hi(sb01));
  p = pack2(bf_lo(p) + bf_lo(q), bf_hi(p) + bf_hi(q));
  q = pack2(bf_lo(k3) * bf_lo(sa01), bf_hi(k3) * bf_lo(sb01));
  p = pack2(bf_lo(p) + bf_lo(q), bf_hi(p) + bf_hi(q));
  return pack2(bf_lo(p) * bf_lo(v), bf_hi(p) * bf_hi(v));
}
DI u32 lo2(u32 a, u32 b) { return __builtin_amdgcn_perm(b, a, 0x05040100u); }
DI u32 hi2(u32 a, u32 b) { return __builtin_amdgcn_perm(b, a, 0x07060302u); }

__global__ __launch_bounds__(1024, 4) void fused_kernel(const u16* __restrict__ qin, const u16* __restrict__ kin, const u16* __restrict__ vin,
                                                         const u16* __restrict__ shk, const u16* __restrict__ ssmk, const u16* __restrict__ ssmD,
                                                         const u16* __restrict__ x, const u16* __restrict__ wqkv, const u16* __restrict__ wo,
                                                         char* __restrict__ ws, u16* __restrict__ out) {
  __shared__ __attribute__((aligned(1024))) char lds[LDS_BYTES];
  const u32 lds0 = (u32)(uintptr_t)lds;
  int tid = threadIdx.x, lane = tid & 63;
  const int wid = __builtin_amdgcn_readfirstlane(tid >> 6);
  int l15 = lane & 15, g = lane >> 4;
#define FRESH_IDS() do { asm volatile("" : "+v"(tid)); lane = tid & 63; l15 = lane & 15; g = lane >> 4; } while (0)
  const int b = blockIdx.x, xcd = b & 7, idx = b >> 3;
  const rsrc_t wr = __builtin_amdgcn_make_buffer_rsrc(ws, 0, WS_END, 0x00020000);
  STAMP_DECL; STAMP(0);
  u32x2 tag;
  const int jr = xcd >> 1, jc = xcd & 1;
  const bool has_gemm = !(jc == 1 && idx >= 28);
  const int rt = jr * 4 + (idx & 3), ct = jc * 8 + (idx >> 2);
  const int cbase = 80 * xcd + 5 * (idx >> 1), odd = idx & 1;
  const int ch0 = cbase + (odd ? 3 : 0), ch2 = cbase + 2;

  if (wid < 8) {
    {
      const int it = tid < 320 ? tid : tid - 192;
      const int cq = it % 20, rg = it / 20;
      const int s0 = idx * 64 + rg * 4, c0 = xcd * 80 + cq * 4;
      u32x2 kk[7], vv[4];
      u32x4 sh0, sh1;
      if (wid < 5) {
        UNR for (int j = 0; j < 7; ++j) { const int r = s0 - 3 + j; kk[j] = __builtin_nontemporal_load((const u32x2*)(kin + (size_t)(r < 0 ? 0 : r) * 640 + c0)); }
        UNR for (int j = 0; j < 4; ++j) vv[j] = __builtin_nontemporal_load((const u32x2*)(vin + (size_t)(s0 + j) * 640 + c0));
        sh0 = *(const u32x4*)(shk + c0 * 4); sh1 = *(const u32x4*)(shk + c0 * 4 + 8);
      }
      const u32x2 ep = ld64_sc1(wr, WS_EPOCH);
      if (tid >= 320 && tid < 320 + C_NUM) *(LDS3 u32*)(uintptr_t)(lds0 + L_CNT + (tid - 320) * 4) = 0u;
      if (tid >= 384 && tid < 480) lds_w128(lds0 + L_XR + ((tid - 384) >> 5) * XR_CH + 4096 + (tid & 31) * 16, u32x4{0u, 0u, 0u, 0u});
      LG_BAR();
      if (wid < 5) {
        if (s0 == 0) { kk[0] = u32x2{0u, 0u}; kk[1] = u32x2{0u, 0u}; kk[2] = u32x2{0u, 0u}; }
        u32 r01[4], r23[4];
        UNR for (int j = 0; j < 4; ++j) {
          r01[j] = kv_pair(kk[j][0], kk[j + 1][0], kk[j + 2][0], kk[j + 3][0], vv[j][0], sh0[0], sh0[1], sh0[2], sh0[3]);
          r23[j] = kv_pair(kk[j][1], kk[j + 1][1], kk[j + 2][1], kk[j + 3][1], vv[j][1], sh1[0], sh1[1], sh1[2], sh1[3]);
        }
        const u32 a = lds0 + L_KV + (cq * 4) * 144 + (60 - 4 * rg) * 2;
        *(LDS3 u32x2*)(uintptr_t)(a) = u32x2{lo2(r01[3], r01[2]), lo2(r01[1], r01[0])};
        *(LDS3 u32x2*)(uintptr_t)(a + 144) = u32x2{hi2(r01[3], r01[2]), hi2(r01[1], r01[0])};
        *(LDS3 u32x2*)(uintptr_t)(a + 288) = u32x2{lo2(r23[3], r23[2]), lo2(r23[1], r23[0])};
        *(LDS3 u32x2*)(uintptr_t)(a + 432) = u32x2{hi2(r23[3], r23[2]), hi2(r23[1], r23[0])};
      }
      const u64 e = (((u64)ep[1] << 32) | ep[0]) + 1;
      tag = u32x2{(u32)__builtin_amdgcn_readfirstlane((u32)e), (u32)__builtin_amdgcn_readfirstlane((u32)(e >> 32))};
      if (tid == 448) *(LDS3 u32x2*)(uintptr_t)(lds0 + L_TAG) = tag;
      STAMP(11);
      cnt_sig(lds0, C_S1); cnt_wait(lds0, C_S1, 8);
      STAMP(12);
      st128_sc1(wr, WS_XR + (xcd * 80 + (tid >> 3)) * 4096 + (1984 - 64 * idx) * 2 + (tid & 7) * 16, lds_r128(lds0 + L_KV + (tid >> 3) * 144 + (tid & 7) * 16));
      if (tid < 128) st128_sc1(wr, WS_XR + (xcd * 80 + 64 + (tid >> 3)) * 4096 + (1984 - 64 * idx) * 2 + (tid & 7) * 16,
                               lds_r128(lds0 + L_KV + (64 + (tid >> 3)) * 144 + (tid & 7) * 16));
      LG_BAR();
      STAMP(13);
    }
    FRESH_IDS();
    if (has_gemm) {
      if (ct < 10) gemm1_compute<true>(lds0, wr, wid, lane, rt, ct, WS_F1 + (xcd * 32 + idx) * FSTRIDE, tag);
      else gemm1_compute<false>(lds0, wr, wid, lane, rt, ct, WS_F1 + (xcd * 32 + idx) * FSTRIDE, tag);
    } else {
      LG_BAR(); LG_BAR(); VM_BAR(0);
      if (wid == 0) flag_publish(wr, WS_F1 + (xcd * 32 + idx) * FSTRIDE, tag);
      UNR for (int t = 4; t < 10; ++t) LG_BAR();
    }
    STAMP(1);
    VM0();
    if (cnt_add(lds0, C_B) == 7 && has_gemm) flag_publish(wr, WS_F2 + (rt * 15 + ct) * FSTRIDE, tag);
    STAMP(2);
    FRESH_IDS();
    const int head = xcd * 4 + (idx >> 3), qb = idx & 7, pr = head >> 1, g0 = 2 * (head & 1);
    if (wid == 0) {
      const int which = lane >> 3, r = (lane >> 1) & 3, hi = lane & 1;
      const int prt = 2 * qb - 2 + r, pct = 5 * which + ((20 * head + (hi ? 19 : 0)) >> 7);
      const bool need = lane < 24 && prt >= 0 && !(which == 0 && r < 2);
      flags_wait(wr, WS_F2 + (need ? (prt * 15 + pct) * FSTRIDE : 0), need, tag);
      cnt_add(lds0, C_QKV);
    }
    cnt_wait(lds0, C_B, 8);
    if (qb == 0) {
      lds_w128(lds0 + L_AK + tid * 16, u32x4{0u, 0u, 0u, 0u});
      if (tid < 256) lds_w128(lds0 + L_AK + 8192 + tid * 16, u32x4{0u, 0u, 0u, 0u});
      lds_w128(lds0 + L_AV + tid * 16, u32x4{0u, 0u, 0u, 0u});
      if (tid < 128) lds_w128(lds0 + L_AV + 8192 + tid * 16, u32x4{0u, 0u, 0u, 0u});
    }
    if (tid < 16) { const u32 o1 = (qb > 0 || tid >= 8) ? 0x3f803f80u : 0u; lds_w128(lds0 + L_ONES + tid * 16, u32x4{o1, o1, o1, o1}); }
    cnt_wait(lds0, C_QKV, 1);
    STAMP(3);
    if (qb > 0 || wid >= 4) {
      UNR for (int j = 0; j < 3; ++j) {
        const int s = 64 * j + lane, i = s / 48, u = s - i * 48;
        glds16_sc1(ws + WS_K + (size_t)(((pr * 128 + qb * 16 - 16 + 4 * wid + i) * 5 + g0) * 256 + u * 16), lds0 + L_AK + (3 * wid + j) * 1024);
      }
    }
    UNR for (int j = 0; j < 3; ++j) {
      const int n = wid + 8 * j;
      if (n < 20 && (qb > 0 || n >= 10)) glds16_sc1(ws + WS_V + (size_t)((head * 64 + qb * 8 - 8) * 1280 + n * 1024 + lane * 16), lds0 + L_AV + n * 1024);
    }
    const int qt = qb * 16 + wid * 2, gq = g < 3 ? g : 2;
    u32x4 qA = ld128_sc1(wr, WS_Q + ((pr * 128 + qt) * 5 + g0 + gq) * 256 + l15 * 16);
    u32x4 qB = ld128_sc1(wr, WS_Q + ((pr * 128 + qt + 1) * 5 + g0 + gq) * 256 + l15 * 16);
    {
      const bool zlo = g == 3 || ((head & 1) && g == 0), zhi = g == 3 || (!(head & 1) && g == 2);
      if (zlo) { qA[0] = 0u; qA[1] = 0u; qB[0] = 0u; qB[1] = 0u; }
      if (zhi) { qA[2] = 0u; qA[3] = 0u; qB[2] = 0u; qB[3] = 0u; }
    }
    VM0();
    cnt_add(lds0, C_KV);
    cnt_wait(lds0, C_KV, 8);
    STAMP(4);
    {
      const u32 ka = lds0 + L_AK + wid * 1536 + gq * 256 + l15 * 16;
      const u32 va0 = lds0 + L_AV + wid * 1280 + g * 256 + l15 * 16;
      const u32 va1 = l15 < 4 ? lds0 + L_AV + wid * 1280 + 1024 + g * 64 + l15 * 16 : lds0 + L_ONES + wid * 16;
      const u32 v1inc = l15 < 4 ? 1280u : 16u;
      AttnOut o;
      attn_fast(ka, va0, va1, v1inc, qA, qB, l15, g, o);
      float lA = __shfl(o.oA1[0], 16 + l15), lB = __shfl(o.oB1[0], 16 + l15);
      if (__any(!(lA <= 16777216.f) || !(lB <= 16777216.f))) {
        attn_slow(ka, va0, va1, v1inc, qA, qB, qb == 0 ? 8 - wid : 0, l15, g, o);
        lA = __shfl(o.oA1[0], 16 + l15); lB = __shfl(o.oB1[0], 16 + l15);
      }
      const float iA = 1.0f / lA, iB = 1.0f / lB;
      const u32x4 v = swap_pack(pack2(o.oA0[0] * iA, o.oA0[1] * iA), pack2(o.oA0[2] * iA, o.oA0[3] * iA),
                                pack2(o.oB0[0] * iB, o.oB0[1] * iB), pack2(o.oB0[2] * iB, o.oB0[3] * iB));
      st128_sc1(wr, WS_O + (qt + (g & 1)) * 20480 + (5 * pr + g0 + (g >> 1)) * 256 + l15 * 16, v);
      const u32x4 w = swap_pack(pack2(o.oA1[0] * iA, o.oA1[1] * iA), pack2(o.oA1[2] * iA, o.oA1[3] * iA),
                                pack2(o.oB1[0] * iB, o.oB1[1] * iB), pack2(o.oB1[2] * iB, o.oB1[3] * iB));
      if (g < 2) st64_sc1(wr, WS_O + (qt + g) * 20480 + (5 * pr + 4) * 256 + l15 * 16 + (head & 1) * 8, u32x2{w[0], w[1]});
    }
    STAMP(5);
    VM0();
    if (cnt_add(lds0, C_O) == 7) flag_publish(wr, WS_F4 + (qb * 32 + head) * FSTRIDE, tag);
    STAMP(6);
    FRESH_IDS();
    const int rb = xcd * 4 + (idx & 3), cb = idx >> 2;
    if (wid == 0) {
      flags_wait(wr, WS_F4 + (xcd * 32 + (lane & 31)) * FSTRIDE, true, tag);
      cnt_add(lds0, C_OOK);
    }
    cnt_wait(lds0, C_OOK, 1);
    STAMP(7);
    {
      const int mt = wid & 3, kh = wid >> 2;
      u32x4 of[10];
      UNR for (int ks = 0; ks < 10; ++ks) of[ks] = ld128_sc1(wr, WS_O + (rb * 4 + mt) * 20480 + (kh * 10 + ks) * 1024 + lane * 16);
      cnt_wait(lds0, C_W, 8);
      cnt_wait(lds0, C_O, 8);
      STAMP(8);
      const u32 wa = lds0 + L_WO + kh * 51200 + (16 * (g >> 1) + 4 * (g & 1) + (l15 >> 2)) * 160 + (lane & 3) * 8;
      f32x4 acc[5];
      UNR for (int j = 0; j < 5; ++j) acc[j] = f32x4{0.f, 0.f, 0.f, 0.f};
      UNR for (int ks = 0; ks < 10; ++ks) {
        UNR for (int j = 0; j < 5; ++j) {
          const u32x2 lo = lds_tr(wa + ks * 5120 + j * 32), hi = lds_tr(wa + ks * 5120 + 1280 + j * 32);
          acc[j] = mfma16(u32x4{lo[0], lo[1], hi[0], hi[1]}, of[ks], acc[j]);
        }
      }
      STAMP(9);
      const u32 red = lds0 + L_RED3 + mt * 5120 + lane * 16;
      if (kh == 0) { lds_w128(red + 2 * 1024, __builtin_bit_cast(u32x4, acc[2])); lds_w128(red + 3 * 1024, __builtin_bit_cast(u32x4, acc[3])); }
      else { lds_w128(red, __builtin_bit_cast(u32x4, acc[0])); lds_w128(red + 1024, __builtin_bit_cast(u32x4, acc[1])); lds_w128(red + 4 * 1024, __builtin_bit_cast(u32x4, acc[4])); }
      cnt_add(lds0, C_R0 + mt);
      cnt_wait(lds0, C_R0 + mt, 2);
      u16* orow = out + (size_t)(rb * 64 + mt * 16 + l15) * 1280 + 640 + cb * 80;
      if (kh == 0) {
        const f32x4 a = acc[0] + __builtin_bit_cast(f32x4, lds_r128(red)), c = acc[1] + __builtin_bit_cast(f32x4, lds_r128(red + 1024));
        const f32x4 e = acc[4] + __builtin_bit_cast(f32x4, lds_r128(red + 4 * 1024));
        *(u32x4*)(orow + (g & 1) * 16 + (g >> 1) * 8) = swap_pack(pack2(a[0], a[1]), pack2(a[2], a[3]), pack2(c[0], c[1]), pack2(c[2], c[3]));
        *(u32x2*)(orow + 64 + 4 * g) = u32x2{pack2(e[0], e[1]), pack2(e[2], e[3])};
      } else {
        const f32x4 a = acc[2] + __builtin_bit_cast(f32x4, lds_r128(red + 2 * 1024)), c = acc[3] + __builtin_bit_cast(f32x4, lds_r128(red + 3 * 1024));
        *(u32x4*)(orow + 32 + (g & 1) * 16 + (g >> 1) * 8) = swap_pack(pack2(a[0], a[1]), pack2(a[2], a[3]), pack2(c[0], c[1]), pack2(c[2], c[3]));
      }
    }
    STAMP(10);
  } else {
    {
      const int aw = wid - 8, t = tid - 512, tc = t & 255;
      u32x4 hd[2]; u32x2 he[2];
      UNR for (int c = 0; c < 2; ++c) {
        const u16* hp = ssmk + (size_t)(c == 0 ? ch0 + (t >> 8) : ch2) * 2048 + tc * 8;
        const u16* hq = hp + (tc < 255 ? 8 : 0);
        asm volatile("global_load_dwordx4 %0, %1, off nt" : "=v"(hd[c]) : "v"(hp) : "memory");
        asm volatile("global_load_dwordx2 %0, %1, off nt" : "=v"(he[c]) : "v"(hq) : "memory");
      }
#define H_WRITE(i, c) do { const u32x4 d = hd[i]; const u32x2 e = he[i]; \
        const u32 a01 = __builtin_amdgcn_alignbit(d[1], d[0], 16), a12 = __builtin_amdgcn_alignbit(d[2], d[1], 16), a23 = __builtin_amdgcn_alignbit(d[3], d[2], 16), \
                  a34 = __builtin_amdgcn_alignbit(e[0], d[3], 16), a45 = __builtin_amdgcn_alignbit(e[1], e[0], 16); \
        const u32 a = lds0 + L_HC + (c) * HC_CH + 32 + tc * 16; \
        lds_w128(a, d); lds_w128(a + HC_STRIDE, u32x4{a01, a12, a23, a34}); \
        lds_w128(a + 2 * HC_STRIDE, u32x4{d[1], d[2], d[3], e[0]}); lds_w128(a + 3 * HC_STRIDE, u32x4{a12, a23, a34, a45}); \
        if (tc == 0) { const u32 z = 0u; \
          lds_w128(a - 32, u32x4{z, z, z, z}); lds_w128(a - 16, u32x4{z, z, z, z}); \
          lds_w128(a - 32 + HC_STRIDE, u32x4{z, z, z, z}); lds_w128(a - 16 + HC_STRIDE, u32x4{z, z, z, d[0] << 16}); \
          lds_w128(a - 32 + 2 * HC_STRIDE, u32x4{z, z, z, z}); lds_w128(a - 16 + 2 * HC_STRIDE, u32x4{z, z, z, d[0]}); \
          lds_w128(a - 32 + 3 * HC_STRIDE, u32x4{z, z, z, z}); lds_w128(a - 16 + 3 * HC_STRIDE, u32x4{z, z, d[0] << 16, a01}); } } while (0)
#define H_PIN() asm volatile("" : "+v"(hd[0]), "+v"(hd[1]), "+v"(he[0]), "+v"(he[1]))
      u32 touch = 0;
      u32x2 f1 = u32x2{0u, 0u};
      const char* f1p = ws + WS_F1 + (xcd * 32 + (lane & 31)) * FSTRIDE;
      asm volatile("s_barrier" ::: "memory");
      {
        const int lid = idx * 512 + t;
        const char* tp;
        if (lid < 5120) tp = (const char*)x + (size_t)jr * 512 * 1280 + (size_t)lid * 128;
        else { const int l2 = lid - 5120; int row = l2 >> 4, c = l2 & 15; row = row > 639 ? 639 : row; c = (jc == 1 && c > 13) ? 13 : c;
               tp = (const char*)wqkv + (size_t)row * 3840 + jc * 2048 + c * 128; }
        asm volatile("global_load_dword %0, %1, off" : "=v"(touch) : "v"(tp) : "memory");
      }
      asm volatile("s_waitcnt vmcnt(1)" : "+v"(hd[0]), "+v"(hd[1]), "+v"(he[0]), "+v"(he[1]) :: "memory");
      if (t < 256) { H_WRITE(0, 0); H_WRITE(1, 2); } else H_WRITE(0, 1);
      if (has_gemm) {
        const char* xs = (const char*)x + (size_t)(rt * 128 + aw * 16 + (lane >> 3)) * 1280 + (((lane & 7) ^ (lane >> 3)) * 16);
        const char* ws0 = (const char*)wqkv + (size_t)(aw * 8 + g) * 3840 + ct * 256 + ((l15 ^ ((g << 2) | ((2 * aw) & 3))) * 16);
        const char* ws1 = (const char*)wqkv + (size_t)(aw * 8 + 4 + g) * 3840 + ct * 256 + ((l15 ^ ((g << 2) | ((2 * aw + 1) & 3))) * 16);
        const u32 xd = lds0 + aw * 2048, wd = lds0 + 16384 + aw * 2048;
#define L_ISSUE(kt) do { const u32 sb_ = ((kt) % 3) * L_STAGE; \
          glds16(xs + (kt) * 128, xd + sb_); glds16(ws0 + (size_t)(kt) * 64 * 3840, wd + sb_); \
          glds16(xs + 8 * 1280 + (kt) * 128, xd + sb_ + 1024); glds16(ws1 + (size_t)(kt) * 64 * 3840, wd + sb_ + 1024); } while (0)
        L_ISSUE(0); L_ISSUE(1);
        STAMP(1);
        VM_BAR(4); STAMP(2); CSTAMP(13); L_ISSUE(2);
        VM_BAR(4); L_ISSUE(3);
        VM_BAR(4); L_ISSUE(4);
        VM_BAR(4); L_ISSUE(5);
        VM_BAR(4); STAMP(3); L_ISSUE(6);
        VM_BAR(4); L_ISSUE(7);
        VM_BAR(4); L_ISSUE(8);
        VM_BAR(4); L_ISSUE(9);
        asm volatile("global_load_dwordx2 %0, %1, off sc1" : "=v"(f1) : "v"(f1p) : "memory");
        VM_BAR(5);
        VM_BAR(1);
        STAMP(12); CSTAMP(14);
      } else {
        UNR for (int i = 0; i < 8; ++i) LG_BAR();
        asm volatile("global_load_dwordx2 %0, %1, off sc1" : "=v"(f1) : "v"(f1p) : "memory");
        LG_BAR(); LG_BAR();
      }
      VM0();
      asm volatile("" : "+v"(touch), "+v"(f1));
      { const u32x2 tg = lds_r64v(lds0 + L_TAG); tag = u32x2{(u32)__builtin_amdgcn_readfirstlane(tg[0]), (u32)__builtin_amdgcn_readfirstlane(tg[1])}; }
      if (wid == 8) {
        if (!flags_ok(f1, tag, true)) flags_wait(wr, WS_F1 + (xcd * 32 + (lane & 31)) * FSTRIDE, true, tag);
        cnt_sig(lds0, C_S2);
      }
      cnt_wait(lds0, C_S2, 1);
      UNR for (int i = 0; i < 2; ++i) {
        const int j = aw + 8 * i, c = j >> 2, p = j & 3;
        if (j < 12) glds16_sc1(ws + WS_XR + (size_t)((c < 2 ? ch0 + c : ch2) * 4096 + p * 1024 + lane * 16), lds0 + L_XR + c * XR_CH + p * 1024);
      }
      VM0();
      cnt_sig(lds0, C_XR);
    }
    STAMP(4);
    FRESH_IDS();
    {
      const int cw = wid - 8;
      const int c = cw < 4 ? (cw >> 1) : 2;
      const int chn = c < 2 ? ch0 + c : ch2;
      const float dcoef = bf_lo((u32)ssmD[chn]);
      const u32 xb = lds0 + L_XR + c * XR_CH;
      const u32 ha = lds0 + L_HC + c * HC_CH + ((l15 + 1) & 3) * HC_STRIDE + (8 * g + ((l15 + 1) & ~3)) * 2;
      const u32 xa = xb + 480 - 32 * l15 + 16 * g;
      const u32 yrow = WS_Y2 + chn * 4096;
      f32x4 acc[8];
      UNR for (int i = 0; i < 8; ++i) acc[i] = f32x4{0.f, 0.f, 0.f, 0.f};
      cnt_wait(lds0, C_XR, 8);
      STAMP(5);
      if (cw < 4) {
        const int hf = cw & 1;
        conv_accum<0xFF, 4>(ha + hf * 256, xa + hf * 256, acc);
        STAMP(6);
        const u32 red = lds0 + L_RED1 + c * 8192 + lane * 16;
        cnt_wait(lds0, C_B, 8);
        if (hf == 0) { UNR for (int i = 0; i < 4; ++i) lds_w128(red + i * 1024, __builtin_bit_cast(u32x4, acc[4 + i])); }
        else { UNR for (int i = 0; i < 4; ++i) lds_w128(red + 4096 + i * 1024, __builtin_bit_cast(u32x4, acc[i])); }
        cnt_add(lds0, C_P0 + c);
        cnt_wait(lds0, C_P0 + c, 2);
        u32x2 y[4];
        if (hf == 0) { UNR for (int i = 0; i < 4; ++i) y[i] = conv_fin(acc[i] + __builtin_bit_cast(f32x4, lds_r128(red + 4096 + i * 1024)), i, xb, dcoef, l15, g); }
        else { UNR for (int i = 0; i < 4; ++i) y[i] = conv_fin(acc[4 + i] + __builtin_bit_cast(f32x4, lds_r128(red + i * 1024)), 4 + i, xb, dcoef, l15, g); }
        conv_store2(wr, yrow, y[0], 4 * hf, y[1], 4 * hf + 1, l15, g);
        conv_store2(wr, yrow, y[2], 4 * hf + 2, y[3], 4 * hf + 3, l15, g);
      } else {
        const int qr = cw - 4;
        const u32 red = lds0 + L_RED2 + lane * 16;
        if (!odd) {
          conv_accum<0xC4, 2>(ha + qr * 128, xa + qr * 128, acc);
          STAMP(6);
          cnt_wait(lds0, C_B, 8);
          const int tl[3] = {7, 6, 2};
          UNR for (int j = 0; j < 3; ++j) if (qr != j) lds_w128(red + (j * 3 + (qr > j ? qr - 1 : qr)) * 1024, __builtin_bit_cast(u32x4, acc[tl[j]]));
          cnt_add(lds0, C_Q4);
          cnt_wait(lds0, C_Q4, 4);
          UNR for (int j = 0; j < 3; ++j) if (qr == j) {
            f32x4 a = acc[tl[j]];
            UNR for (int w = 0; w < 3; ++w) a += __builtin_bit_cast(f32x4, lds_r128(red + (j * 3 + w) * 1024));
            conv_store1(wr, yrow, conv_fin(a, tl[j], xb, dcoef, l15, g), tl[j], l15, g);
          }
        } else {
          conv_accum<0x3B, 2>(ha + qr * 128, xa + qr * 128, acc);
          STAMP(6);
          cnt_wait(lds0, C_B, 8);
          const int tl[5] = {5, 4, 3, 1, 0};
          UNR for (int j = 0; j < 5; ++j) { const int ow = j & 3; if (qr != ow) lds_w128(red + (j * 3 + (qr > ow ? qr - 1 : qr)) * 1024, __builtin_bit_cast(u32x4, acc[tl[j]])); }
          cnt_add(lds0, C_Q4);
          cnt_wait(lds0, C_Q4, 4);
          UNR for (int j = 0; j < 5; ++j) if (qr == (j & 3)) {
            f32x4 a = acc[tl[j]];
            UNR for (int w = 0; w < 3; ++w) a += __builtin_bit_cast(f32x4, lds_r128(red + (j * 3 + w) * 1024));
            conv_store1(wr, yrow, conv_fin(a, tl[j], xb, dcoef, l15, g), tl[j], l15, g);
          }
        }
      }
    }
    STAMP(7);
    VM0();
    if (cnt_add(lds0, C_Y) == 7) flag_publish(wr, WS_F3 + (xcd * 32 + idx) * FSTRIDE, tag);
    cnt_wait(lds0, C_Y, 8);
    STAMP(8);
    FRESH_IDS();
    {
      const int aw = wid - 8, cb = idx >> 2;
      for (int n = aw; n < 100; n += 8) {
        const int s = n * 64 + lane, rho = s / 10, ck = s - rho * 10;
        const int r = rho & 31, G = 4 * (rho >> 5) + 2 * (r >> 4) + ((r >> 2) & 1), j = 4 * ((r >> 3) & 1) + (r & 3);
        const int i = G / 5, u = G - i * 5;
        const int krow = 40 * i + (u < 4 ? 20 * (u >> 1) + 8 * (u & 1) + j : (j < 4 ? 16 + j : 32 + j));
        glds16((const char*)wo + (size_t)krow * 1280 + cb * 160 + ck * 16, lds0 + L_WO + n * 1024);
      }
    }
    {
      const int aw = wid - 8, t = tid - 512;
      const int rgp = aw & 3, cs = aw >> 2;
      const int row = idx * 64 + rgp * 16 + l15;
      const int ck0 = cs * 4 + g, ck1 = 8 + (g & 1);
      const u32x4 q0 = __builtin_nontemporal_load((const u32x4*)(qin + (size_t)row * 640 + xcd * 80 + ck0 * 8));
      const u32x4 q1 = __builtin_nontemporal_load((const u32x4*)(qin + (size_t)row * 640 + xcd * 80 + ck1 * 8));
      VM0();
      cnt_sig(lds0, C_W);
      if (wid == 8) {
        flags_wait(wr, WS_F3 + (xcd * 32 + (lane & 31)) * FSTRIDE, true, tag);
        cnt_add(lds0, C_YOK);
      }
      cnt_wait(lds0, C_YOK, 1);
      STAMP(9);
      const u32x4 y0 = ld128_sc1(wr, WS_Y2 + (xcd * 80 + (t >> 3)) * 4096 + idx * 128 + (t & 7) * 16);
      const int t2 = 512 + (t & 127);
      const u32x4 y1 = ld128_sc1(wr, WS_Y2 + (xcd * 80 + (t2 >> 3)) * 4096 + idx * 128 + (t2 & 7) * 16);
      lds_w128(lds0 + L_GT + (t >> 3) * 144 + (t & 7) * 16, y0);
      if (t < 128) lds_w128(lds0 + L_GT + (t2 >> 3) * 144 + (t2 & 7) * 16, y1);
      cnt_add(lds0, C_G);
      cnt_wait(lds0, C_G, 8);
      STAMP(10);
      const u32 ga = lds0 + L_GT + (l15 >> 2) * 144 + (rgp * 16 + 4 * (lane & 3)) * 2;
      u32x2 a0 = lds_tr(ga + ck0 * 8 * 144), a1 = lds_tr(ga + ck0 * 8 * 144 + 4 * 144);
      u32x2 b0 = lds_tr(ga + ck1 * 8 * 144), b1 = lds_tr(ga + ck1 * 8 * 144 + 4 * 144);
      asm volatile("" : "+v"(a0), "+v"(a1), "+v"(b0), "+v"(b1));
      *(u32x4*)(out + (size_t)row * 1280 + xcd * 80 + ck0 * 8) =
          u32x4{pack2(bf_lo(q0[0]) * bf_lo(a0[0]), bf_hi(q0[0]) * bf_hi(a0[0])), pack2(bf_lo(q0[1]) * bf_lo(a0[1]), bf_hi(q0[1]) * bf_hi(a0[1])),
                pack2(bf_lo(q0[2]) * bf_lo(a1[0]), bf_hi(q0[2]) * bf_hi(a1[0])), pack2(bf_lo(q0[3]) * bf_lo(a1[1]), bf_hi(q0[3]) * bf_hi(a1[1]))};
      if (cs == 0 && g < 2)
        *(u32x4*)(out + (size_t)row * 1280 + xcd * 80 + ck1 * 8) =
            u32x4{pack2(bf_lo(q1[0]) * bf_lo(b0[0]), bf_hi(q1[0]) * bf_hi(b0[0])), pack2(bf_lo(q1[1]) * bf_lo(b0[1]), bf_hi(q1[1]) * bf_hi(b0[1])),
                  pack2(bf_lo(q1[2]) * bf_lo(b1[0]), bf_hi(q1[2]) * bf_hi(b1[0])), pack2(bf_lo(q1[3]) * bf_lo(b1[1]), bf_hi(q1[3]) * bf_hi(b1[1]))};
    }
    STAMP(11);
    if (b == 0 && wid == 15) {
      UNR for (int j = 0; j < 4; ++j) flags_wait(wr, WS_F1 + (j * 64 + lane) * FSTRIDE, true, tag);
      flag_publish(wr, WS_EPOCH, tag);
    }
  }
  STAMP(15);
  STAMP_FLUSH(ws);
}

extern "C" void kernel_launch(void* const* d_in, const int* in_sizes, int n_in, void* d_out, int out_size, void* d_ws, size_t ws_size,
                              hipStream_t stream) {
  fused_kernel<<<256, 1024, 0, stream>>>((const u16*)d_in[0], (const u16*)d_in[1], (const u16*)d_in[2], (const u16*)d_in[3], (const u16*)d_in[4],
                                         (const u16*)d_in[5], (const u16*)d_in[6], (const u16*)d_in[7], (const u16*)d_in[8], (char*)d_ws, (u16*)d_out);
}
```

```cpp
#include <hip/hip_runtime.h>
#include <hip/hip_bf16.h>
#include <stdint.h>

typedef unsigned short u16;
typedef unsigned int u32;
typedef unsigned long long u64;
typedef __bf16 bf16x8 __attribute__((ext_vector_type(8)));
typedef __bf16 bf16x2 __attribute__((ext_vector_type(2)));
typedef float f32x4 __attribute__((ext_vector_type(4)));
typedef float f32x2 __attribute__((ext_vector_type(2)));
typedef u32 u32x2 __attribute__((ext_vector_type(2)));
typedef u32 u32x4 __attribute__((ext_vector_type(4)));
typedef short s16x4 __attribute__((ext_vector_type(4)));
typedef __amdgpu_buffer_rsrc_t rsrc_t;
#define LDS3 __attribute__((address_space(3)))
#define DI __device__ __forceinline__
#define UNR _Pragma("unroll")
#define SBAR() __builtin_amdgcn_sched_barrier(0)

constexpr u32 PLANE = 2048u * 640u * 2u;
constexpr u32 WS_XR = 0;
constexpr u32 WS_Q = 1 * PLANE;
constexpr u32 WS_K = 2 * PLANE;
constexpr u32 WS_V = 3 * PLANE;
constexpr u32 WS_O = 4 * PLANE;
constexpr u32 WS_Y2 = 5 * PLANE;
constexpr u32 WS_EPOCH = 6 * PLANE;
#define FSTRIDE 128
constexpr u32 WS_F1 = WS_EPOCH + 2048, WS_F2 = WS_F1 + 320 * FSTRIDE, WS_F3 = WS_F2 + 256 * FSTRIDE, WS_F4 = WS_F3 + 256 * FSTRIDE;
constexpr u32 WS_END = WS_F4 + 256 * FSTRIDE;
constexpr float QSCALE = 0.22360679774997896f * 1.4426950408889634f;

constexpr u32 L_STAGE = 32768, L_KV = 65536, L_HC = 98304, HC_STRIDE = 4160, HC_CH = 16640, L_XR = 148224, XR_CH = 4608, L_CNT = 162048, LDS_BYTES = 162304;
constexpr u32 L_AK = 0, L_AV = 24576, L_ONES = 45056, L_RED1 = 49152, L_RED2 = 65536;
constexpr u32 L_TAG = L_CNT + 240;
constexpr u32 L_WO = 45312, L_GT = 148224, L_RED3 = 0;
enum { C_B = 0, C_QKV, C_KV, C_O, C_OOK, C_XR, C_P0, C_P1, C_Q4, C_Y, C_YOK, C_G, C_W, C_R0, C_R1, C_R2, C_R3, C_S1, C_S2, C_H, C_TAG,
       C_FULL = 24, C_EMPTY = 36, C_TAGV = 48, C_NUM = 52 };
constexpr u32 SPIN_CAP = 1u << 22;
#define POLL_SLEEP 4

#define STAMP_DECL
#define STAMP(i)
#define CSTAMP(i)
#define STAMP_FLUSH(wsp)

DI float bf_lo(u32 v) { return __uint_as_float(v << 16); }
DI float bf_hi(u32 v) { return __uint_as_float(v & 0xffff0000u); }
DI u32 pack2(float lo, float hi) { f32x2 v = {lo, hi}; return __builtin_bit_cast(u32, __builtin_convertvector(v, bf16x2)); }
DI f32x4 mfma16(u32x4 a, u32x4 b, f32x4 c) {
  return __builtin_amdgcn_mfma_f32_16x16x32_bf16(__builtin_bit_cast(bf16x8, a), __builtin_bit_cast(bf16x8, b), c, 0, 0, 0);
}
#define ST_AUX 16
#define LD_AUX 16
#define LD_POL "sc1"
DI void glds16(const void* g, u32 lds_base) {
  u32 sv;
  asm volatile("s_mov_b32 %0, m0\n\ts_mov_b32 m0, %2\n\ts_nop 0\n\tglobal_load_lds_dwordx4 %1, off\n\ts_mov_b32 m0, %0"
               : "=&s"(sv) : "v"(g), "s"(lds_base) : "memory");
}
DI void glds16_sc1(const void* g, u32 lds_base) {
  u32 sv;
  asm volatile("s_mov_b32 %0, m0\n\ts_mov_b32 m0, %2\n\ts_nop 0\n\tglobal_load_lds_dwordx4 %1, off " LD_POL "\n\ts_mov_b32 m0, %0"
               : "=&s"(sv) : "v"(g), "s"(lds_base) : "memory");
}
DI u32x4 lds_r128(u32 a) { return *(const LDS3 u32x4*)(uintptr_t)a; }
DI u32x2 lds_r64(u32 a) { return *(const LDS3 u32x2*)(uintptr_t)a; }
DI u32x2 lds_r64v(u32 a) { return *(const volatile LDS3 u32x2*)(uintptr_t)a; }
DI void lds_w128(u32 a, u32x4 v) { *(LDS3 u32x4*)(uintptr_t)a = v; }
DI u32x2 lds_tr(u32 a) { return __builtin_bit_cast(u32x2, __builtin_amdgcn_ds_read_tr16_b64_v4i16((LDS3 s16x4*)(uintptr_t)a)); }
DI void st128_sc1(rsrc_t r, u32 off, u32x4 v) { __builtin_amdgcn_raw_buffer_store_b128(v, r, off, 0, ST_AUX); }
DI void st64_sc1(rsrc_t r, u32 off, u32x2 v) { __builtin_amdgcn_raw_buffer_store_b64(v, r, off, 0, 16); }
DI u32x4 ld128_sc1(rsrc_t r, u32 off) { return __builtin_amdgcn_raw_buffer_load_b128(r, off, 0, LD_AUX); }
DI u32x2 ld64_sc1(rsrc_t r, u32 off) { return __builtin_amdgcn_raw_buffer_load_b64(r, off, 0, 16); }
#define VM_BAR(N) asm volatile("s_waitcnt vmcnt(" #N ") lgkmcnt(0)\n\ts_barrier" ::: "memory")
#define LG_BAR() asm volatile("s_waitcnt lgkmcnt(0)\n\ts_barrier" ::: "memory")
#define VM0() asm volatile("s_waitcnt vmcnt(0)" ::: "memory")

DI u32 cnt_add(u32 lds0, int c) {
  asm volatile("s_waitcnt lgkmcnt(0)" ::: "memory");
  u32 old = 0;
  if ((threadIdx.x & 63) == 0) old = __hip_atomic_fetch_add((LDS3 u32*)(uintptr_t)(lds0 + L_CNT + 4 * c), 1u, __ATOMIC_RELAXED, __HIP_MEMORY_SCOPE_WORKGROUP);
  return __builtin_amdgcn_readfirstlane(old);
}
DI void cnt_sig(u32 lds0, int c) {
  asm volatile("s_waitcnt lgkmcnt(0)" ::: "memory");
  if ((threadIdx.x & 63) == 0) (void)__hip_atomic_fetch_add((LDS3 u32*)(uintptr_t)(lds0 + L_CNT + 4 * c), 1u, __ATOMIC_RELAXED, __HIP_MEMORY_SCOPE_WORKGROUP);
  asm volatile("" ::: "memory");
}
DI void cnt_wait(u32 lds0, int c, u32 n) {
  _Pragma("unroll 1") for (u32 spins = 0; spins < SPIN_CAP; ++spins) {
    if (__builtin_amdgcn_readfirstlane(__hip_atomic_load((LDS3 u32*)(uintptr_t)(lds0 + L_CNT + 4 * c), __ATOMIC_RELAXED, __HIP_MEMORY_SCOPE_WORKGROUP)) >= n) break;
    __builtin_amdgcn_s_sleep(1);
  }
  asm volatile("" ::: "memory");
}
DI void flag_publish(rsrc_t r, u32 off, u32x2 tag) { if ((threadIdx.x & 63) == 0) st64_sc1(r, off, tag); }
DI bool flags_ok(u32x2 v, u32x2 tag, bool need) { return __all(!need || (v[0] == tag[0] && v[1] == tag[1])); }
DI void flags_wait(rsrc_t r, u32 off, bool need, u32x2 tag) {
  _Pragma("unroll 1") for (u32 spins = 0; spins < SPIN_CAP; ++spins) {
    asm volatile("" ::: "memory");
    const u32x2 v = ld64_sc1(r, off);
    if (flags_ok(v, tag, need)) break;
    __builtin_amdgcn_s_sleep(POLL_SLEEP);
  }
  asm volatile("" ::: "memory");
}
#define OUT_AUX 2
DI void out128(rsrc_t r, u32 off, u32x4 v) { __builtin_amdgcn_raw_buffer_store_b128(v, r, off, 0, OUT_AUX); }
DI void out64(rsrc_t r, u32 off, u32x2 v) { __builtin_amdgcn_raw_buffer_store_b64(v, r, off, 0, OUT_AUX); }
DI u32x4 swap_pack(u32 a0, u32 a1, u32 b0, u32 b1) {
  auto s0 = __builtin_amdgcn_permlane16_swap(a0, b0, false, false);
  auto s1 = __builtin_amdgcn_permlane16_swap(a1, b1, false, false);
  return u32x4{s0[0], s1[0], s0[1], s1[1]};
}
DI float xmax4(float m) {
  auto r = __builtin_amdgcn_permlane32_swap(__float_as_uint(m), __float_as_uint(m), false, false);
  m = fmaxf(__uint_as_float(r[0]), __uint_as_float(r[1]));
  auto s = __builtin_amdgcn_permlane16_swap(__float_as_uint(m), __float_as_uint(m), false, false);
  return fmaxf(__uint_as_float(s[0]), __uint_as_float(s[1]));
}
DI float ex2(float v) { return __builtin_amdgcn_exp2f(v); }

template <bool SWAP>
DI void gemm1_compute(u32 lds0, rsrc_t wr, int wid, int lane, int rt, int ct, u32 f1off, u32x2 tag) {
  asm volatile("" : "+v"(lane));
  const int l15 = lane & 15, g = lane >> 4, wm = wid >> 2, wn = wid & 3;
  const u32 xa0 = lds0 + (wm * 64 + l15) * 128 + ((g ^ (lane & 7)) * 16), xa1 = xa0 ^ 64;
  u32 wa[2][2];
  {
    const int q = l15 >> 2, p = lane & 3;
    UNR for (int nt = 0; nt < 2; ++nt) UNR for (int t = 0; t < 2; ++t) {
      const int swz = (q << 2) | ((2 * g + t) & 3);
      wa[nt][t] = lds0 + 16384 + 256 * (8 * g + 4 * t + q) + 16 * ((2 * (wn * 2 + nt) + (p >> 1)) ^ swz) + 8 * (p & 1);
    }
  }
  f32x4 acc[4][2];
  UNR for (int i = 0; i < 4; ++i) UNR for (int j = 0; j < 2; ++j) acc[i][j] = f32x4{0.f, 0.f, 0.f, 0.f};
  u32x4 xf[2][4], wf[2][2];
#define G1_RW(set, kt, ks, nt) do { const u32 sb_ = ((kt) % 3) * L_STAGE; \
    const u32x2 lo = lds_tr(wa[nt][0] + sb_ + (ks) * 8192), hi = lds_tr(wa[nt][1] + sb_ + (ks) * 8192); wf[set][nt] = u32x4{lo[0], lo[1], hi[0], hi[1]}; } while (0)
#define G1_RX(set, kt, ks, mt) xf[set][mt] = lds_r128(((ks) ? xa1 : xa0) + ((kt) % 3) * L_STAGE + (mt) * 2048)
#define G1_M(set, mt, nt) acc[mt][nt] = SWAP ? mfma16(wf[set][nt], xf[set][mt], acc[mt][nt]) : mfma16(xf[set][mt], wf[set][nt], acc[mt][nt])
#define G1_HALF(cs, ns, kt, ks, RD) do { \
    G1_M(cs, 0, 0); if (RD) G1_RW(ns, kt, ks, 0); SBAR(); \
    G1_M(cs, 0, 1); if (RD) { G1_RX(ns, kt, ks, 0); G1_RX(ns, kt, ks, 1); } SBAR(); \
    G1_M(cs, 1, 0); if (RD) G1_RW(ns, kt, ks, 1); SBAR(); \
    G1_M(cs, 1, 1); if (RD) G1_RX(ns, kt, ks, 2); SBAR(); \
    G1_M(cs, 2, 0); if (RD) G1_RX(ns, kt, ks, 3); SBAR(); \
    G1_M(cs, 2, 1); G1_M(cs, 3, 0); G1_M(cs, 3, 1); SBAR(); } while (0)
  G1_RW(0, 0, 0, 0); G1_RX(0, 0, 0, 0); G1_RW(0, 0, 0, 1); G1_RX(0, 0, 0, 1); G1_RX(0, 0, 0, 2); G1_RX(0, 0, 0, 3);
  SBAR();
  UNR for (int t = 0; t < 10; ++t) {
    G1_HALF(0, 1, t, 1, true);
    if (t < 9) {
      if (t == 2) VM_BAR(0); else LG_BAR();
      if (t == 2 && wid == 0) flag_publish(wr, f1off, tag);
      SBAR();
    }
    G1_HALF(1, 0, t + 1, 0, t < 9);
  }
  if (SWAP) {
    const float sc = ct < 5 ? QSCALE : 1.0f;
    const u32 base = ct < 5 ? WS_Q : WS_K;
    UNR for (int nt = 0; nt < 2; ++nt) {
      const int G8 = ((ct % 5) * 128 + wn * 32 + nt * 16) / 8 + (g >> 1);
      const int pr = G8 / 5, gr = G8 - pr * 5;
      UNR for (int mp = 0; mp < 2; ++mp) {
        const f32x4 a = acc[2 * mp][nt], b = acc[2 * mp + 1][nt];
        const u32x4 v = swap_pack(pack2(a[0] * sc, a[1] * sc), pack2(a[2] * sc, a[3] * sc), pack2(b[0] * sc, b[1] * sc), pack2(b[2] * sc, b[3] * sc));
        const int tile = rt * 8 + wm * 4 + 2 * mp + (g & 1);
        st128_sc1(wr, base + ((pr * 128 + tile) * 5 + gr) * 256 + l15 * 16, v);
      }
    }
  } else {
    UNR for (int nt = 0; nt < 2; ++nt) {
      const int c = (ct - 10) * 128 + wn * 32 + nt * 16 + l15;
      const int h = c / 20, d = c - h * 20;
      const int off = d < 16 ? g * 256 + d * 16 : 1024 + g * 64 + (d - 16) * 16;
      UNR for (int mp = 0; mp < 2; ++mp) {
        const int blk = rt * 4 + wm * 2 + mp;
        const f32x4 a = acc[2 * mp][nt], b = acc[2 * mp + 1][nt];
        st128_sc1(wr, WS_V + (h * 64 + blk) * 1280 + off, u32x4{pack2(a[0], a[1]), pack2(a[2], a[3]), pack2(b[0], b[1]), pack2(b[2], b[3])});
      }
    }
  }
}

struct AttnOut { f32x4 oA0, oA1, oB0, oB1; };

DI void attn_slow(u32 ka, u32 va0, u32 va1, u32 v1inc, u32x4 qA, u32x4 qB, int kb0, int l15, int g, AttnOut& o) {
  const f32x4 z = f32x4{0.f, 0.f, 0.f, 0.f};
  float tA = -INFINITY, tB = -INFINITY;
  for (int t = 2 * kb0; t < 18; ++t) {
    const u32x4 kf = lds_r128(ka + t * 768);
    const f32x4 sA = mfma16(kf, qA, z), sB = mfma16(kf, qB, z);
    UNR for (int r = 0; r < 4; ++r) {
      const int dA = 16 * t + 4 * g + r - l15;
      tA = fmaxf(tA, (dA >= 1 && dA <= 256) ? sA[r] : -INFINITY);
      tB = fmaxf(tB, (dA >= 17 && dA <= 272) ? sB[r] : -INFINITY);
    }
  }
  const float mA = xmax4(tA), mB = xmax4(tB);
  o.oA0 = o.oA1 = o.oB0 = o.oB1 = z;
  for (int kbk = kb0; kbk < 9; ++kbk) {
    float pa[8], pb[8];
    UNR for (int hf = 0; hf < 2; ++hf) {
      const int t = 2 * kbk + hf;
      const u32x4 kf = lds_r128(ka + t * 768);
      const f32x4 sA = mfma16(kf, qA, z), sB = mfma16(kf, qB, z);
      UNR for (int r = 0; r < 4; ++r) {
        const int dA = 16 * t + 4 * g + r - l15;
        pa[4 * hf + r] = (dA >= 1 && dA <= 256) ? ex2(sA[r] - mA) : 0.f;
        pb[4 * hf + r] = (dA >= 17 && dA <= 272) ? ex2(sB[r] - mB) : 0.f;
      }
    }
    const u32x4 vf0 = lds_r128(va0 + kbk * 1280), vf1 = lds_r128(va1 + kbk * v1inc);
    const u32x4 pA = u32x4{pack2(pa[0], pa[1]), pack2(pa[2], pa[3]), pack2(pa[4], pa[5]), pack2(pa[6], pa[7])};
    const u32x4 pB = u32x4{pack2(pb[0], pb[1]), pack2(pb[2], pb[3]), pack2(pb[4], pb[5]), pack2(pb[6], pb[7])};
    o.oA0 = mfma16(vf0, pA, o.oA0); o.oA1 = mfma16(vf1, pA, o.oA1);
    o.oB0 = mfma16(vf0, pB, o.oB0); o.oB1 = mfma16(vf1, pB, o.oB1);
  }
}

DI void attn_fast(u32 ka, u32 va0, u32 va1, u32 v1inc, u32x4 qA, u32x4 qB, int l15, int g, AttnOut& o) {
  const f32x4 z = f32x4{0.f, 0.f, 0.f, 0.f};
  bool lo[4];
  UNR for (int r = 0; r < 4; ++r) lo[r] = 4 * g + r <= l15;
  u32x4 kf[2][2], vf[2][2], pA, pB;
  f32x4 S[2][4];
  kf[1][0] = lds_r128(ka + 16 * 768); kf[1][1] = lds_r128(ka + 17 * 768);
  kf[0][0] = lds_r128(ka); kf[0][1] = lds_r128(ka + 768);
  vf[1][0] = lds_r128(va0 + 8 * 1280); vf[1][1] = lds_r128(va1 + 8 * v1inc);
  f32x4 cA, cB;
  {
    const f32x4 sA = mfma16(kf[1][0], qA, z), sB = mfma16(kf[1][1], qB, z), sB0 = mfma16(kf[1][0], qB, z);
    SBAR();
    kf[1][0] = lds_r128(ka + 2 * 768); kf[1][1] = lds_r128(ka + 3 * 768);
    float mA = -INFINITY, mB = -INFINITY;
    UNR for (int r = 0; r < 4; ++r) { mA = fmaxf(mA, lo[r] ? sA[r] : -INFINITY); mB = fmaxf(mB, lo[r] ? sB[r] : -INFINITY); }
    mA = xmax4(mA); mB = xmax4(mB);
    cA = f32x4{-mA, -mA, -mA, -mA}; cB = f32x4{-mB, -mB, -mB, -mB};
    S[0][0] = mfma16(kf[0][0], qA, cA); S[0][1] = mfma16(kf[0][1], qA, cA); S[0][3] = mfma16(kf[0][1], qB, cB);
    float a[4], b0[4], b1[4];
    UNR for (int r = 0; r < 4; ++r) { a[r] = lo[r] ? ex2(sA[r] - mA) : 0.f; b0[r] = ex2(sB0[r] - mB); b1[r] = lo[r] ? ex2(sB[r] - mB) : 0.f; }
    pA = u32x4{pack2(a[0], a[1]), pack2(a[2], a[3]), 0u, 0u};
    pB = u32x4{pack2(b0[0], b0[1]), pack2(b0[2], b0[3]), pack2(b1[0], b1[1]), pack2(b1[2], b1[3])};
  }
  o.oA0 = o.oA1 = o.oB0 = o.oB1 = z;
  SBAR();
  UNR for (int k = 0; k < 8; ++k) {
    const int c = k & 1, n = c ^ 1;
    vf[c][0] = lds_r128(va0 + k * 1280); vf[c][1] = lds_r128(va1 + k * v1inc);
    if (k < 6) { kf[c][0] = lds_r128(ka + (2 * k + 4) * 768); kf[c][1] = lds_r128(ka + (2 * k + 5) * 768); }
    SBAR();
    u32 np[8];
    UNR for (int i = 0; i < 4; ++i) {
      if (k < 7) S[n][i] = mfma16(kf[n][i & 1], i < 2 ? qA : qB, i < 2 ? cA : cB);
      float e[4];
      if (k == 0 && i == 2) { np[4] = 0u; np[5] = 0u; }
      else {
        e[0] = ex2(S[c][i][0]); e[1] = ex2(S[c][i][1]);
      }
      if (i == 0) o.oA0 = mfma16(vf[n][0], pA, o.oA0);
      if (i == 1) o.oA1 = mfma16(vf[n][1], pA, o.oA1);
      if (i == 2) o.oB0 = mfma16(vf[n][0], pB, o.oB0);
      if (i == 3) o.oB1 = mfma16(vf[n][1], pB, o.oB1);
      if (!(k == 0 && i == 2)) {
        e[2] = ex2(S[c][i][2]); e[3] = ex2(S[c][i][3]);
        if (k == 0 && (i == 0 || i == 3)) { UNR for (int r = 0; r < 4; ++r) e[r] = lo[r] ? 0.f : e[r]; }
        np[2 * i] = pack2(e[0], e[1]); np[2 * i + 1] = pack2(e[2], e[3]);
      }
      SBAR();
    }
    pA = u32x4{np[0], np[1], np[2], np[3]}; pB = u32x4{np[4], np[5], np[6], np[7]};
  }
  o.oA0 = mfma16(vf[1][0], pA, o.oA0); o.oA1 = mfma16(vf[1][1], pA, o.oA1);
  o.oB0 = mfma16(vf[1][0], pB, o.oB0); o.oB1 = mfma16(vf[1][1], pB, o.oB1);
}

template <int TMASK, int NR>
DI void conv_accum(u32 ha, u32 xa, f32x4 (&acc)[8]) {
  constexpr int NF = (TMASK & 0xC0) ? 8 : 6;
  u32x4 A[8], B[8];
#define CV_LA(i, rr) do { if ((i) < NF) { const u32x2 lo_ = lds_r64v(ha + (rr) * 64 + (i) * 512), hi_ = lds_r64v(ha + (rr) * 64 + (i) * 512 + 8); \
    A[i] = u32x4{lo_[0], lo_[1], hi_[0], hi_[1]}; } } while (0)
#define CV_LB(m, rr) do { if ((m) < NF) B[m] = lds_r128(xa + (rr) * 64 + (7 - (m)) * 512); } while (0)
#define CV_MM(i, m) do { if ((i) < NF && (m) < NF && (i) + (m) < 8 && ((TMASK >> ((i) + (m))) & 1)) acc[(i) + (m)] = mfma16(A[i], B[m], acc[(i) + (m)]); } while (0)
  CV_LB(0, 0); UNR for (int i = 7; i >= 4; --i) CV_LA(i, 0);
  UNR for (int m = 1; m < 4; ++m) CV_LB(m, 0);
  UNR for (int i = 0; i < 4; ++i) { CV_LA(i, 0); CV_LB(7 - i, 0); }
  SBAR();
  UNR for (int rr = 0; rr < NR; ++rr) {
    UNR for (int m = 0; m < 4; ++m) UNR for (int i = 7; i >= 4; --i) CV_MM(i, m);
    SBAR();
    if (rr + 1 < NR) { UNR for (int i = 7; i >= 4; --i) CV_LA(i, rr + 1); }
    SBAR();
    UNR for (int i = 0; i < 4; ++i) UNR for (int m = 7; m >= 4; --m) CV_MM(i, m);
    SBAR();
    if (rr + 1 < NR) { UNR for (int m = 7; m >= 4; --m) CV_LB(m, rr + 1); }
    SBAR();
    UNR for (int m = 0; m < 4; ++m) {
      UNR for (int i = 0; i < 4; ++i) CV_MM(i, m);
      SBAR();
      if (rr + 1 < NR) CV_LB(m, rr + 1);
      SBAR();
    }
    if (rr + 1 < NR) { UNR for (int i = 0; i < 4; ++i) CV_LA(i, rr + 1); }
    SBAR();
  }
}
DI u32x2 conv_fin(f32x4 a, int n, u32 xbase, float dcoef, int l15, int g) {
  const int s = 256 * n + 16 * l15 + 4 * g;
  const u32x2 xv = lds_r64(xbase + (2044 - s) * 2);
  const u32 y01 = pack2(a[0], a[1]), y23 = pack2(a[2], a[3]);
  const u32 d01 = pack2(bf_hi(xv[1]) * dcoef, bf_lo(xv[1]) * dcoef), d23 = pack2(bf_hi(xv[0]) * dcoef, bf_lo(xv[0]) * dcoef);
  return u32x2{pack2(bf_lo(y01) + bf_lo(d01), bf_hi(y01) + bf_hi(d01)), pack2(bf_lo(y23) + bf_lo(d23), bf_hi(y23) + bf_hi(d23))};
}
DI void conv_store2(rsrc_t wr, u32 rowoff, u32x2 ya, int na, u32x2 yb, int nb, int l15, int g) {
  const u32x4 v = swap_pack(ya[0], ya[1], yb[0], yb[1]);
  st128_sc1(wr, rowoff + (256 * ((g & 1) ? nb : na) + 16 * l15 + 8 * (g >> 1)) * 2, v);
}
DI void conv_store1(rsrc_t wr, u32 rowoff, u32x2 ya, int na, int l15, int g) { st64_sc1(wr, rowoff + (256 * na + 16 * l15 + 4 * g) * 2, ya); }

DI u32 kv_pair(u32 k0, u32 k1, u32 k2, u32 k3, u32 v, u32 sa01, u32 sa23, u32 sb01, u32 sb23) {
  u32 p = pack2(bf_lo(k0) * bf_hi(sa23), bf_hi(k0) * bf_hi(sb23));
  u32 q = pack2(bf_lo(k1) * bf_lo(sa23), bf_hi(k1) * bf_lo(sb23));
  p = pack2(bf_lo(p) + bf_lo(q), bf_hi(p) + bf_hi(q));
  q = pack2(bf_lo(k2) * bf_hi(sa01), bf_hi(k2) * bf_hi(sb01));
  p = pack2(bf_lo(p) + bf_lo(q), bf_hi(p) + bf_hi(q));
  q = pack2(bf_lo(k3) * bf_lo(sa01), bf_hi(k3) * bf_lo(sb01));
  p = pack2(bf_lo(p) + bf_lo(q), bf_hi(p) + bf_hi(q));
  return pack2(bf_lo(p) * bf_lo(v), bf_hi(p) * bf_hi(v));
}
DI u32 lo2(u32 a, u32 b) { return __builtin_amdgcn_perm(b, a, 0x05040100u); }
DI u32 hi2(u32 a, u32 b) { return __builtin_amdgcn_perm(b, a, 0x07060302u); }

__global__ __launch_bounds__(1024, 4) void fused_kernel(const u16* __restrict__ qin, const u16* __restrict__ kin, const u16* __restrict__ vin,
                                                         const u16* __restrict__ shk, const u16* __restrict__ ssmk, const u16* __restrict__ ssmD,
                                                         const u16* __restrict__ x, const u16* __restrict__ wqkv, const u16* __restrict__ wo,
                                                         char* __restrict__ ws, u16* __restrict__ out) {
  __shared__ __attribute__((aligned(1024))) char lds[LDS_BYTES];
  const u32 lds0 = (u32)(uintptr_t)lds;
  int tid = threadIdx.x, lane = tid & 63;
  const int wid = __builtin_amdgcn_readfirstlane(tid >> 6);
  int l15 = lane & 15, g = lane >> 4;
#define FRESH_IDS() do { asm volatile("" : "+v"(tid)); lane = tid & 63; l15 = lane & 15; g = lane >> 4; } while (0)
  const int b = blockIdx.x, xcd = b & 7, idx = b >> 3;
  const rsrc_t wr = __builtin_amdgcn_make_buffer_rsrc(ws, 0, WS_END, 0x00020000);
  const rsrc_t orr = __builtin_amdgcn_make_buffer_rsrc(out, 0, 2048 * 1280 * 2, 0x00020000);
  STAMP_DECL; STAMP(0);
  u32 code_touch = 0;
  if (wid == 15) {
    u64 pc; u32 len;
    asm volatile("s_getpc_b64 %0\n.Lpf_here:\n\ts_mov_b32 %1, .Lpf_end-.Lpf_here" : "=s"(pc), "=s"(len));
    const u32 off = (u32)(idx * 16 + (lane & 15)) * 128u;
    const __attribute__((address_space(1))) u32* cp = (const __attribute__((address_space(1))) u32*)(pc + (off + 4 <= len ? off : 0u));
    code_touch = *cp;
  }
  u32x2 tag;
  const int jr = xcd >> 1, jc = xcd & 1;
  const bool has_gemm = !(jc == 1 && idx >= 28);
  const int rt = jr * 4 + (idx & 3), ct = jc * 8 + (idx >> 2);
  const int cbase = 80 * xcd + 5 * (idx >> 1), odd = idx & 1;
  const int ch0 = cbase + (odd ? 3 : 0), ch2 = cbase + 2;

  if (wid < 8) {
    {
      const int it = tid < 320 ? tid : tid - 192;
      const int cq = it % 20, rg = it / 20;
      const int s0 = idx * 64 + rg * 4, c0 = xcd * 80 + cq * 4;
      u32x2 kk[7], vv[4];
      u32x4 sh0, sh1;
      if (wid < 5) {
        UNR for (int j = 0; j < 7; ++j) { const int r = s0 - 3 + j; kk[j] = __builtin_nontemporal_load((const u32x2*)(kin + (size_t)(r < 0 ? 0 : r) * 640 + c0)); }
        UNR for (int j = 0; j < 4; ++j) vv[j] = __builtin_nontemporal_load((const u32x2*)(vin + (size_t)(s0 + j) * 640 + c0));
        sh0 = *(const u32x4*)(shk + c0 * 4); sh1 = *(const u32x4*)(shk + c0 * 4 + 8);
      }
      const u32x2 ep = ld64_sc1(wr, WS_EPOCH);
      if (tid >= 320 && tid < 320 + C_NUM) *(LDS3 u32*)(uintptr_t)(lds0 + L_CNT + (tid - 320) * 4) = 0u;
      if (tid >= 384 && tid < 480) lds_w128(lds0 + L_XR + ((tid - 384) >> 5) * XR_CH + 4096 + (tid & 31) * 16, u32x4{0u, 0u, 0u, 0u});
      LG_BAR();
      if (wid < 5) {
        if (s0 == 0) { kk[0] = u32x2{0u, 0u}; kk[1] = u32x2{0u, 0u}; kk[2] = u32x2{0u, 0u}; }
        u32 r01[4], r23[4];
        UNR for (int j = 0; j < 4; ++j) {
          r01[j] = kv_pair(kk[j][0], kk[j + 1][0], kk[j + 2][0], kk[j + 3][0], vv[j][0], sh0[0], sh0[1], sh0[2], sh0[3]);
          r23[j] = kv_pair(kk[j][1], kk[j + 1][1], kk[j + 2][1], kk[j + 3][1], vv[j][1], sh1[0], sh1[1], sh1[2], sh1[3]);
        }
        const u32 a = lds0 + L_KV + (cq * 4) * 144 + (60 - 4 * rg) * 2;
        *(LDS3 u32x2*)(uintptr_t)(a) = u32x2{lo2(r01[3], r01[2]), lo2(r01[1], r01[0])};
        *(LDS3 u32x2*)(uintptr_t)(a + 144) = u32x2{hi2(r01[3], r01[2]), hi2(r01[1], r01[0])};
        *(LDS3 u32x2*)(uintptr_t)(a + 288) = u32x2{lo2(r23[3], r23[2]), lo2(r23[1], r23[0])};
        *(LDS3 u32x2*)(uintptr_t)(a + 432) = u32x2{hi2(r23[3], r23[2]), hi2(r23[1], r23[0])};
      }
      const u64 e = (((u64)ep[1] << 32) | ep[0]) + 1;
      tag = u32x2{(u32)__builtin_amdgcn_readfirstlane((u32)e), (u32)__builtin_amdgcn_readfirstlane((u32)(e >> 32))};
      if (tid == 448) *(LDS3 u32x2*)(uintptr_t)(lds0 + L_TAG) = tag;
      STAMP(11);
      cnt_sig(lds0, C_S1); cnt_wait(lds0, C_S1, 8);
      STAMP(12);
      st128_sc1(wr, WS_XR + (xcd * 80 + (tid >> 3)) * 4096 + (1984 - 64 * idx) * 2 + (tid & 7) * 16, lds_r128(lds0 + L_KV + (tid >> 3) * 144 + (tid & 7) * 16));
      if (tid < 128) st128_sc1(wr, WS_XR + (xcd * 80 + 64 + (tid >> 3)) * 4096 + (1984 - 64 * idx) * 2 + (tid & 7) * 16,
                               lds_r128(lds0 + L_KV + (64 + (tid >> 3)) * 144 + (tid & 7) * 16));
      LG_BAR();
      STAMP(13);
      FRESH_IDS();
      if (has_gemm) {
        if (ct < 10) gemm1_compute<true>(lds0, wr, wid, lane, rt, ct, WS_F1 + (xcd * 32 + idx) * FSTRIDE, tag);
        else gemm1_compute<false>(lds0, wr, wid, lane, rt, ct, WS_F1 + (xcd * 32 + idx) * FSTRIDE, tag);
      } else {
        LG_BAR(); LG_BAR(); VM_BAR(0);
        if (wid == 0) flag_publish(wr, WS_F1 + (xcd * 32 + idx) * FSTRIDE, tag);
        UNR for (int t = 4; t < 10; ++t) LG_BAR();
      }
    }
    STAMP(1);
    VM0();
    if (cnt_add(lds0, C_B) == 7 && has_gemm) flag_publish(wr, WS_F2 + (rt * 15 + ct) * FSTRIDE, tag);
    STAMP(2);
    FRESH_IDS();
    const int head = xcd * 4 + (idx >> 3), qb = idx & 7, pr = head >> 1, g0 = 2 * (head & 1);
    if (wid == 0) {
      const int which = lane >> 3, r = (lane >> 1) & 3, hi = lane & 1;
      const int prt = 2 * qb - 2 + r, pct = 5 * which + ((20 * head + (hi ? 19 : 0)) >> 7);
      const bool need = lane < 24 && prt >= 0 && !(which == 0 && r < 2);
      flags_wait(wr, WS_F2 + (need ? (prt * 15 + pct) * FSTRIDE : 0), need, tag);
      cnt_add(lds0, C_QKV);
    }
    cnt_wait(lds0, C_B, 8);
    if (qb == 0) {
      lds_w128(lds0 + L_AK + tid * 16, u32x4{0u, 0u, 0u, 0u});
      if (tid < 256) lds_w128(lds0 + L_AK + 8192 + tid * 16, u32x4{0u, 0u, 0u, 0u});
      lds_w128(lds0 + L_AV + tid * 16, u32x4{0u, 0u, 0u, 0u});
      if (tid < 128) lds_w128(lds0 + L_AV + 8192 + tid * 16, u32x4{0u, 0u, 0u, 0u});
    }
    if (tid < 16) { const u32 o1 = (qb > 0 || tid >= 8) ? 0x3f803f80u : 0u; lds_w128(lds0 + L_ONES + tid * 16, u32x4{o1, o1, o1, o1}); }
    cnt_wait(lds0, C_QKV, 1);
    STAMP(3);
    if (qb > 0 || wid >= 4) {
      UNR for (int j = 0; j < 3; ++j) {
        const int s = 64 * j + lane, i = s / 48, u = s - i * 48;
        glds16_sc1(ws + WS_K + (size_t)(((pr * 128 + qb * 16 - 16 + 4 * wid + i) * 5 + g0) * 256 + u * 16), lds0 + L_AK + (3 * wid + j) * 1024);
      }
    }
    UNR for (int j = 0; j < 3; ++j) {
      const int n = wid + 8 * j;
      if (n < 20 && (qb > 0 || n >= 10)) glds16_sc1(ws + WS_V + (size_t)((head * 64 + qb * 8 - 8) * 1280 + n * 1024 + lane * 16), lds0 + L_AV + n * 1024);
    }
    const int qt = qb * 16 + wid * 2, gq = g < 3 ? g : 2;
    u32x4 qA = ld128_sc1(wr, WS_Q + ((pr * 128 + qt) * 5 + g0 + gq) * 256 + l15 * 16);
    u32x4 qB = ld128_sc1(wr, WS_Q + ((pr * 128 + qt + 1) * 5 + g0 + gq) * 256 + l15 * 16);
    {
      const bool zlo = g == 3 || ((head & 1) && g == 0), zhi = g == 3 || (!(head & 1) && g == 2);
      if (zlo) { qA[0] = 0u; qA[1] = 0u; qB[0] = 0u; qB[1] = 0u; }
      if (zhi) { qA[2] = 0u; qA[3] = 0u; qB[2] = 0u; qB[3] = 0u; }
    }
    VM0();
    cnt_add(lds0, C_KV);
    cnt_wait(lds0, C_KV, 8);
    STAMP(4);
    u32 tw;
    {
      const char* p1 = (const char*)wo + (size_t)(idx * 200 + (tid < 200 ? tid : 0)) * 128;
      const char* p2 = (const char*)qin + (size_t)(idx * 64 + ((tid & 127) >> 1)) * 1280 + xcd * 160 + (tid & 1) * 128;
      tw = *(const u32*)(tid < 256 ? p1 : p2);
    }
    {
      const u32 ka = lds0 + L_AK + wid * 1536 + gq * 256 + l15 * 16;
      const u32 va0 = lds0 + L_AV + wid * 1280 + g * 256 + l15 * 16;
      const u32 va1 = l15 < 4 ? lds0 + L_AV + wid * 1280 + 1024 + g * 64 + l15 * 16 : lds0 + L_ONES + wid * 16;
      const u32 v1inc = l15 < 4 ? 1280u : 16u;
      AttnOut o;
      attn_fast(ka, va0, va1, v1inc, qA, qB, l15, g, o);
      float lA = __shfl(o.oA1[0], 16 + l15), lB = __shfl(o.oB1[0], 16 + l15);
      if (__any(!(lA <= 16777216.f) || !(lB <= 16777216.f))) {
        attn_slow(ka, va0, va1, v1inc, qA, qB, qb == 0 ? 8 - wid : 0, l15, g, o);
        lA = __shfl(o.oA1[0], 16 + l15); lB = __shfl(o.oB1[0], 16 + l15);
      }
      const float iA = 1.0f / lA, iB = 1.0f / lB;
      const u32x4 v = swap_pack(pack2(o.oA0[0] * iA, o.oA0[1] * iA), pack2(o.oA0[2] * iA, o.oA0[3] * iA),
                                pack2(o.oB0[0] * iB, o.oB0[1] * iB), pack2(o.oB0[2] * iB, o.oB0[3] * iB));
      st128_sc1(wr, WS_O + (qt + (g & 1)) * 20480 + (5 * pr + g0 + (g >> 1)) * 256 + l15 * 16, v);
      const u32x4 w = swap_pack(pack2(o.oA1[0] * iA, o.oA1[1] * iA), pack2(o.oA1[2] * iA, o.oA1[3] * iA),
                                pack2(o.oB1[0] * iB, o.oB1[1] * iB), pack2(o.oB1[2] * iB, o.oB1[3] * iB));
      if (g < 2) st64_sc1(wr, WS_O + (qt + g) * 20480 + (5 * pr + 4) * 256 + l15 * 16 + (head & 1) * 8, u32x2{w[0], w[1]});
    }
    STAMP(5);
    VM0();
    asm volatile("" :: "v"(tw));
    if (cnt_add(lds0, C_O) == 7) flag_publish(wr, WS_F4 + (qb * 32 + head) * FSTRIDE, tag);
    STAMP(6);
    FRESH_IDS();
    const int rb = xcd * 4 + (idx & 3), cb = idx >> 2;
    if (wid == 0) {
      flags_wait(wr, WS_F4 + (xcd * 32 + (lane & 31)) * FSTRIDE, true, tag);
      cnt_add(lds0, C_OOK);
    }
    cnt_wait(lds0, C_OOK, 1);
    STAMP(7);
    {
      const int mt = wid & 3, kh = wid >> 2;
      u32x4 of[10];
      UNR for (int ks = 0; ks < 10; ++ks) of[ks] = ld128_sc1(wr, WS_O + (rb * 4 + mt) * 20480 + (kh * 10 + ks) * 1024 + lane * 16);
      cnt_wait(lds0, C_W, 8);
      cnt_wait(lds0, C_O, 8);
      STAMP(8);
      const u32 wa = lds0 + L_WO + kh * 51200 + (16 * (g >> 1) + 4 * (g & 1) + (l15 >> 2)) * 160 + (lane & 3) * 8;
      f32x4 acc[5];
      UNR for (int j = 0; j < 5; ++j) acc[j] = f32x4{0.f, 0.f, 0.f, 0.f};
      UNR for (int ks = 0; ks < 10; ++ks) {
        UNR for (int j = 0; j < 5; ++j) {
          const u32x2 lo = lds_tr(wa + ks * 5120 + j * 32), hi = lds_tr(wa + ks * 5120 + 1280 + j * 32);
          acc[j] = mfma16(u32x4{lo[0], lo[1], hi[0], hi[1]}, of[ks], acc[j]);
        }
      }
      STAMP(9);
      const u32 red = lds0 + L_RED3 + mt * 5120 + lane * 16;
      if (kh == 0) { lds_w128(red + 2 * 1024, __builtin_bit_cast(u32x4, acc[2])); lds_w128(red + 3 * 1024, __builtin_bit_cast(u32x4, acc[3])); }
      else { lds_w128(red, __builtin_bit_cast(u32x4, acc[0])); lds_w128(red + 1024, __builtin_bit_cast(u32x4, acc[1])); lds_w128(red + 4 * 1024, __builtin_bit_cast(u32x4, acc[4])); }
      cnt_add(lds0, C_R0 + mt);
      cnt_wait(lds0, C_R0 + mt, 2);
      const u32 orow = ((rb * 64 + mt * 16 + l15) * 1280 + 640 + cb * 80) * 2;
      if (kh == 0) {
        const f32x4 a = acc[0] + __builtin_bit_cast(f32x4, lds_r128(red)), c = acc[1] + __builtin_bit_cast(f32x4, lds_r128(red + 1024));
        const f32x4 e = acc[4] + __builtin_bit_cast(f32x4, lds_r128(red + 4 * 1024));
        out128(orr, orow + ((g & 1) * 16 + (g >> 1) * 8) * 2, swap_pack(pack2(a[0], a[1]), pack2(a[2], a[3]), pack2(c[0], c[1]), pack2(c[2], c[3])));
        out64(orr, orow + (64 + 4 * g) * 2, u32x2{pack2(e[0], e[1]), pack2(e[2], e[3])});
      } else {
        const f32x4 a = acc[2] + __builtin_bit_cast(f32x4, lds_r128(red + 2 * 1024)), c = acc[3] + __builtin_bit_cast(f32x4, lds_r128(red + 3 * 1024));
        out128(orr, orow + (32 + (g & 1) * 16 + (g >> 1) * 8) * 2, swap_pack(pack2(a[0], a[1]), pack2(a[2], a[3]), pack2(c[0], c[1]), pack2(c[2], c[3])));
      }
    }
    STAMP(10);
  } else {
    {
      const int aw = wid - 8, t = tid - 512, tc = t & 255;
      u32x4 hd[2]; u32x2 he[2];
      UNR for (int c = 0; c < 2; ++c) {
        const u16* hp = ssmk + (size_t)(c == 0 ? ch0 + (t >> 8) : ch2) * 2048 + tc * 8;
        hd[c] = __builtin_nontemporal_load((const u32x4*)hp);
        he[c] = __builtin_nontemporal_load((const u32x2*)(hp + (tc < 255 ? 8 : 0)));
      }
#define H_WRITE(i, c) do { const u32x4 d = hd[i]; const u32x2 e = he[i]; \
        const u32 a01 = __builtin_amdgcn_alignbit(d[1], d[0], 16), a12 = __builtin_amdgcn_alignbit(d[2], d[1], 16), a23 = __builtin_amdgcn_alignbit(d[3], d[2], 16), \
                  a34 = __builtin_amdgcn_alignbit(e[0], d[3], 16), a45 = __builtin_amdgcn_alignbit(e[1], e[0], 16); \
        const u32 a = lds0 + L_HC + (c) * HC_CH + 32 + tc * 16; \
        lds_w128(a, d); lds_w128(a + HC_STRIDE, u32x4{a01, a12, a23, a34}); \
        lds_w128(a + 2 * HC_STRIDE, u32x4{d[1], d[2], d[3], e[0]}); lds_w128(a + 3 * HC_STRIDE, u32x4{a12, a23, a34, a45}); \
        if (tc == 0) { const u32 z = 0u; \
          lds_w128(a - 32, u32x4{z, z, z, z}); lds_w128(a - 16, u32x4{z, z, z, z}); \
          lds_w128(a - 32 + HC_STRIDE, u32x4{z, z, z, z}); lds_w128(a - 16 + HC_STRIDE, u32x4{z, z, z, d[0] << 16}); \
          lds_w128(a - 32 + 2 * HC_STRIDE, u32x4{z, z, z, z}); lds_w128(a - 16 + 2 * HC_STRIDE, u32x4{z, z, z, d[0]}); \
          lds_w128(a - 32 + 3 * HC_STRIDE, u32x4{z, z, z, z}); lds_w128(a - 16 + 3 * HC_STRIDE, u32x4{z, z, d[0] << 16, a01}); } } while (0)
#define H_PIN() asm volatile("" : "+v"(hd[0]), "+v"(hd[1]), "+v"(he[0]), "+v"(he[1]))
      u32 touch = 0;
      u32x2 f1 = u32x2{0u, 0u};
      const u32 f1o = WS_F1 + (xcd * 32 + (lane & 31)) * FSTRIDE;
      bool xr_issued = false;
#define GET_TAG() do { const u32x2 tg = lds_r64v(lds0 + L_TAG); tag = u32x2{(u32)__builtin_amdgcn_readfirstlane(tg[0]), (u32)__builtin_amdgcn_readfirstlane(tg[1])}; } while (0)
#define XR_ISSUE() do { UNR for (int i = 0; i < 2; ++i) { const int j = aw + 8 * i, c = j >> 2, p = j & 3; \
        if (j < 12) glds16_sc1(ws + WS_XR + (size_t)((c < 2 ? ch0 + c : ch2) * 4096 + p * 1024 + lane * 16), lds0 + L_XR + c * XR_CH + p * 1024); } } while (0)
      asm volatile("s_barrier" ::: "memory");
      {
        const int lid = idx * 512 + t;
        const char* tp;
        if (lid < 5120) tp = (const char*)x + (size_t)jr * 512 * 1280 + (size_t)lid * 128;
        else { const int l2 = lid - 5120; int row = l2 >> 4, c = l2 & 15; row = row > 639 ? 639 : row; c = (jc == 1 && c > 13) ? 13 : c;
               tp = (const char*)wqkv + (size_t)row * 3840 + jc * 2048 + c * 128; }
        touch = *(const u32*)tp;
      }
      SBAR();
      if (t < 256) { H_WRITE(0, 0); H_WRITE(1, 2); } else H_WRITE(0, 1);
      if (has_gemm) {
        const char* xs = (const char*)x + (size_t)(rt * 128 + aw * 16 + (lane >> 3)) * 1280 + (((lane & 7) ^ (lane >> 3)) * 16);
        const char* ws0 = (const char*)wqkv + (size_t)(aw * 8 + g) * 3840 + ct * 256 + ((l15 ^ ((g << 2) | ((2 * aw) & 3))) * 16);
        const char* ws1 = (const char*)wqkv + (size_t)(aw * 8 + 4 + g) * 3840 + ct * 256 + ((l15 ^ ((g << 2) | ((2 * aw + 1) & 3))) * 16);
        const u32 xd = lds0 + aw * 2048, wd = lds0 + 16384 + aw * 2048;
#define L_ISSUE(kt) do { const u32 sb_ = ((kt) % 3) * L_STAGE; \
          glds16(xs + (kt) * 128, xd + sb_); glds16(ws0 + (size_t)(kt) * 64 * 3840, wd + sb_); \
          glds16(xs + 8 * 1280 + (kt) * 128, xd + sb_ + 1024); glds16(ws1 + (size_t)(kt) * 64 * 3840, wd + sb_ + 1024); } while (0)
        L_ISSUE(0); L_ISSUE(1);
        STAMP(1);
        VM_BAR(4); STAMP(2); CSTAMP(13); L_ISSUE(2);
        VM_BAR(4); L_ISSUE(3);
        VM_BAR(4); L_ISSUE(4);
        VM_BAR(4); L_ISSUE(5);
        VM_BAR(4); STAMP(3); L_ISSUE(6);
        VM_BAR(4); L_ISSUE(7);
        if (wid == 8) {
          const char* f1p = ws + f1o;
          asm volatile("global_load_dwordx2 %0, %1, off sc1\n\ts_waitcnt vmcnt(5) lgkmcnt(0)\n\ts_barrier" : "=v"(f1) : "v"(f1p) : "memory");
          L_ISSUE(8);
          asm volatile("s_waitcnt vmcnt(4)" : "+v"(f1) :: "memory");
          GET_TAG();
          if (flags_ok(f1, tag, true)) cnt_sig(lds0, C_S2);
          LG_BAR();
        } else {
          VM_BAR(4); L_ISSUE(8);
          VM_BAR(4);
        }
        L_ISSUE(9);
        xr_issued = __builtin_amdgcn_readfirstlane(*(const volatile LDS3 u32*)(uintptr_t)(lds0 + L_CNT + 4 * C_S2)) != 0;
        if (xr_issued) {
          XR_ISSUE();
          if (aw < 4) { VM_BAR(6); VM_BAR(2); } else { VM_BAR(5); VM_BAR(1); }
        } else { VM_BAR(4); VM_BAR(0); }
        STAMP(12); CSTAMP(14);
      } else {
        UNR for (int i = 0; i < 10; ++i) LG_BAR();
      }
      asm volatile("" :: "v"(touch));
      GET_TAG();
      if (!xr_issued) {
        if (wid == 8) { flags_wait(wr, f1o, true, tag); cnt_sig(lds0, C_S2); }
        cnt_wait(lds0, C_S2, 1);
        XR_ISSUE();
      }
      VM0();
      cnt_sig(lds0, C_XR);
    }
    STAMP(4);
    FRESH_IDS();
    {
      const int cw = wid - 8;
      const int c = cw < 4 ? (cw >> 1) : 2;
      const int chn = c < 2 ? ch0 + c : ch2;
      const float dcoef = bf_lo((u32)ssmD[chn]);
      const u32 xb = lds0 + L_XR + c * XR_CH;
      const u32 ha = lds0 + L_HC + c * HC_CH + ((l15 + 1) & 3) * HC_STRIDE + (8 * g + ((l15 + 1) & ~3)) * 2;
      const u32 xa = xb + 480 - 32 * l15 + 16 * g;
      const u32 yrow = WS_Y2 + chn * 4096;
      f32x4 acc[8];
      UNR for (int i = 0; i < 8; ++i) acc[i] = f32x4{0.f, 0.f, 0.f, 0.f};
      cnt_wait(lds0, C_XR, 8);
      STAMP(5);
      if (cw < 4) {
        const int hf = cw & 1;
        conv_accum<0xFF, 4>(ha + hf * 256, xa + hf * 256, acc);
        STAMP(6);
        __builtin_amdgcn_s_setprio(1);
        const u32 red = lds0 + L_RED1 + c * 8192 + lane * 16;
        cnt_wait(lds0, C_B, 8);
        if (hf == 0) { UNR for (int i = 0; i < 4; ++i) lds_w128(red + i * 1024, __builtin_bit_cast(u32x4, acc[4 + i])); }
        else { UNR for (int i = 0; i < 4; ++i) lds_w128(red + 4096 + i * 1024, __builtin_bit_cast(u32x4, acc[i])); }
        cnt_add(lds0, C_P0 + c);
        cnt_wait(lds0, C_P0 + c, 2);
        u32x2 y[4];
        if (hf == 0) { UNR for (int i = 0; i < 4; ++i) y[i] = conv_fin(acc[i] + __builtin_bit_cast(f32x4, lds_r128(red + 4096 + i * 1024)), i, xb, dcoef, l15, g); }
        else { UNR for (int i = 0; i < 4; ++i) y[i] = conv_fin(acc[4 + i] + __builtin_bit_cast(f32x4, lds_r128(red + i * 1024)), 4 + i, xb, dcoef, l15, g); }
        conv_store2(wr, yrow, y[0], 4 * hf, y[1], 4 * hf + 1, l15, g);
        conv_store2(wr, yrow, y[2], 4 * hf + 2, y[3], 4 * hf + 3, l15, g);
      } else {
        const int qr = cw - 4;
        const u32 red = lds0 + L_RED2 + lane * 16;
        if (!odd) {
          conv_accum<0xC4, 2>(ha + qr * 128, xa + qr * 128, acc);
          STAMP(6);
          __builtin_amdgcn_s_setprio(1);
          cnt_wait(lds0, C_B, 8);
          const int tl[3] = {7, 6, 2};
          UNR for (int j = 0; j < 3; ++j) if (qr != j) lds_w128(red + (j * 3 + (qr > j ? qr - 1 : qr)) * 1024, __builtin_bit_cast(u32x4, acc[tl[j]]));
          cnt_add(lds0, C_Q4);
          cnt_wait(lds0, C_Q4, 4);
          UNR for (int j = 0; j < 3; ++j) if (qr == j) {
            f32x4 a = acc[tl[j]];
            UNR for (int w = 0; w < 3; ++w) a += __builtin_bit_cast(f32x4, lds_r128(red + (j * 3 + w) * 1024));
            conv_store1(wr, yrow, conv_fin(a, tl[j], xb, dcoef, l15, g), tl[j], l15, g);
          }
        } else {
          conv_accum<0x3B, 2>(ha + qr * 128, xa + qr * 128, acc);
          STAMP(6);
          __builtin_amdgcn_s_setprio(1);
          cnt_wait(lds0, C_B, 8);
          const int tl[5] = {5, 4, 3, 1, 0};
          UNR for (int j = 0; j < 5; ++j) { const int ow = j & 3; if (qr != ow) lds_w128(red + (j * 3 + (qr > ow ? qr - 1 : qr)) * 1024, __builtin_bit_cast(u32x4, acc[tl[j]])); }
          cnt_add(lds0, C_Q4);
          cnt_wait(lds0, C_Q4, 4);
          UNR for (int j = 0; j < 5; ++j) if (qr == (j & 3)) {
            f32x4 a = acc[tl[j]];
            UNR for (int w = 0; w < 3; ++w) a += __builtin_bit_cast(f32x4, lds_r128(red + (j * 3 + w) * 1024));
            conv_store1(wr, yrow, conv_fin(a, tl[j], xb, dcoef, l15, g), tl[j], l15, g);
          }
        }
      }
    }
    STAMP(7);
    __builtin_amdgcn_s_setprio(0);
    VM0();
    if (cnt_add(lds0, C_Y) == 7) flag_publish(wr, WS_F3 + (xcd * 32 + idx) * FSTRIDE, tag);
    cnt_wait(lds0, C_Y, 8);
    STAMP(8);
    FRESH_IDS();
    {
      const int aw = wid - 8, cb = idx >> 2;
      for (int n = aw; n < 100; n += 8) {
        const int s = n * 64 + lane, rho = s / 10, ck = s - rho * 10;
        const int r = rho & 31, G = 4 * (rho >> 5) + 2 * (r >> 4) + ((r >> 2) & 1), j = 4 * ((r >> 3) & 1) + (r & 3);
        const int i = G / 5, u = G - i * 5;
        const int krow = 40 * i + (u < 4 ? 20 * (u >> 1) + 8 * (u & 1) + j : (j < 4 ? 16 + j : 32 + j));
        glds16((const char*)wo + (size_t)krow * 1280 + cb * 160 + ck * 16, lds0 + L_WO + n * 1024);
      }
    }
    {
      const int aw = wid - 8, t = tid - 512;
      const int rgp = aw & 3, cs = aw >> 2;
      const int row = idx * 64 + rgp * 16 + l15;
      const int ck0 = cs * 4 + g, ck1 = 8 + (g & 1);
      const u32x4 q0 = *(const u32x4*)(qin + (size_t)row * 640 + xcd * 80 + ck0 * 8);
      const u32x4 q1 = *(const u32x4*)(qin + (size_t)row * 640 + xcd * 80 + ck1 * 8);
      VM0();
      cnt_sig(lds0, C_W);
      if (wid == 8) {
        flags_wait(wr, WS_F3 + (xcd * 32 + (lane & 31)) * FSTRIDE, true, tag);
        cnt_add(lds0, C_YOK);
      }
      cnt_wait(lds0, C_YOK, 1);
      STAMP(9);
      const u32x4 y0 = ld128_sc1(wr, WS_Y2 + (xcd * 80 + (t >> 3)) * 4096 + idx * 128 + (t & 7) * 16);
      const int t2 = 512 + (t & 127);
      const u32x4 y1 = ld128_sc1(wr, WS_Y2 + (xcd * 80 + (t2 >> 3)) * 4096 + idx * 128 + (t2 & 7) * 16);
      lds_w128(lds0 + L_GT + (t >> 3) * 144 + (t & 7) * 16, y0);
      if (t < 128) lds_w128(lds0 + L_GT + (t2 >> 3) * 144 + (t2 & 7) * 16, y1);
      cnt_add(lds0, C_G);
      cnt_wait(lds0, C_G, 8);
      STAMP(10);
      const u32 ga = lds0 + L_GT + (l15 >> 2) * 144 + (rgp * 16 + 4 * (lane & 3)) * 2;
      u32x2 a0 = lds_tr(ga + ck0 * 8 * 144), a1 = lds_tr(ga + ck0 * 8 * 144 + 4 * 144);
      u32x2 b0 = lds_tr(ga + ck1 * 8 * 144), b1 = lds_tr(ga + ck1 * 8 * 144 + 4 * 144);
      asm volatile("" : "+v"(a0), "+v"(a1), "+v"(b0), "+v"(b1));
      out128(orr, (row * 1280 + xcd * 80 + ck0 * 8) * 2,
          u32x4{pack2(bf_lo(q0[0]) * bf_lo(a0[0]), bf_hi(q0[0]) * bf_hi(a0[0])), pack2(bf_lo(q0[1]) * bf_lo(a0[1]), bf_hi(q0[1]) * bf_hi(a0[1])),
                pack2(bf_lo(q0[2]) * bf_lo(a1[0]), bf_hi(q0[2]) * bf_hi(a1[0])), pack2(bf_lo(q0[3]) * bf_lo(a1[1]), bf_hi(q0[3]) * bf_hi(a1[1]))});
      if (cs == 0 && g < 2)
        out128(orr, (row * 1280 + xcd * 80 + ck1 * 8) * 2,
            u32x4{pack2(bf_lo(q1[0]) * bf_lo(b0[0]), bf_hi(q1[0]) * bf_hi(b0[0])), pack2(bf_lo(q1[1]) * bf_lo(b0[1]), bf_hi(q1[1]) * bf_hi(b0[1])),
                  pack2(bf_lo(q1[2]) * bf_lo(b1[0]), bf_hi(q1[2]) * bf_hi(b1[0])), pack2(bf_lo(q1[3]) * bf_lo(b1[1]), bf_hi(q1[3]) * bf_hi(b1[1]))});
    }
    STAMP(11);
    if (b == 0 && wid == 15) {
      UNR for (int j = 0; j < 4; ++j) flags_wait(wr, WS_F1 + (j * 64 + lane) * FSTRIDE, true, tag);
      flag_publish(wr, WS_EPOCH, tag);
    }
  }
  STAMP(15);
  STAMP_FLUSH(ws);
  asm volatile(".Lpf_end:" :: "v"(code_touch));
}

extern "C" void kernel_launch(void* const* d_in, const int* in_sizes, int n_in, void* d_out, int out_size, void* d_ws, size_t ws_size,
                              hipStream_t stream) {
  fused_kernel<<<256, 1024, 0, stream>>>((const u16*)d_in[0], (const u16*)d_in[1], (const u16*)d_in[2], (const u16*)d_in[3], (const u16*)d_in[4],
                                         (const u16*)d_in[5], (const u16*)d_in[6], (const u16*)d_in[7], (const u16*)d_in[8], (char*)d_ws, (u16*)d_out);
}
```

```cpp
#include <hip/hip_runtime.h>
#include <hip/hip_bf16.h>
#include <stdint.h>

typedef unsigned short u16;
typedef unsigned int u32;
typedef unsigned long long u64;
typedef __bf16 bf16x8 __attribute__((ext_vector_type(8)));
typedef __bf16 bf16x2 __attribute__((ext_vector_type(2)));
typedef float f32x4 __attribute__((ext_vector_type(4)));
typedef float f32x2 __attribute__((ext_vector_type(2)));
typedef u32 u32x2 __attribute__((ext_vector_type(2)));
typedef u32 u32x4 __attribute__((ext_vector_type(4)));
typedef short s16x4 __attribute__((ext_vector_type(4)));
typedef __amdgpu_buffer_rsrc_t rsrc_t;
#define LDS3 __attribute__((address_space(3)))
#define DI __device__ __forceinline__
#define UNR _Pragma("unroll")
#define SBAR() __builtin_amdgcn_sched_barrier(0)

constexpr u32 PLANE = 2048u * 640u * 2u;
constexpr u32 WS_XR = 0;
constexpr u32 WS_Q = 1 * PLANE;
constexpr u32 WS_K = 2 * PLANE;
constexpr u32 WS_V = 3 * PLANE;
constexpr u32 WS_O = 4 * PLANE;
constexpr u32 WS_Y2 = 5 * PLANE;
constexpr u32 WS_EPOCH = 6 * PLANE;
#define FSTRIDE 128
constexpr u32 WS_F1 = WS_EPOCH + 2048, WS_F2 = WS_F1 + 320 * FSTRIDE, WS_F3 = WS_F2 + 256 * FSTRIDE, WS_F4 = WS_F3 + 256 * FSTRIDE;
constexpr u32 WS_END = WS_F4 + 256 * FSTRIDE;
constexpr float QSCALE = 0.22360679774997896f * 1.4426950408889634f;

constexpr u32 L_STAGE = 32768, L_KV = 65536, L_HC = 98304, HC_STRIDE = 4160, HC_CH = 16640, L_XR = 148224, XR_CH = 4608, L_CNT = 162048, LDS_BYTES = 162304;
constexpr u32 L_AK = 0, L_AV = 24576, L_ONES = 45056, L_RED1 = 49152, L_RED2 = 65536;
constexpr u32 L_TAG = L_CNT + 240;
constexpr u32 L_WO = 45312, L_GT = 148224, L_RED3 = 0;
enum { C_B = 0, C_QKV, C_KV, C_O, C_OOK, C_XR, C_P0, C_P1, C_Q4, C_Y, C_YOK, C_G, C_W, C_R0, C_R1, C_R2, C_R3, C_S1, C_S2, C_H, C_TAG,
       C_FULL = 24, C_EMPTY = 36, C_TAGV = 48, C_NUM = 52 };
constexpr u32 SPIN_CAP = 1u << 22;
#define POLL_SLEEP 4

#define STAMP_DECL
#define STAMP(i)
#define CSTAMP(i)
#define STAMP_FLUSH(wsp)

DI float bf_lo(u32 v) { return __uint_as_float(v << 16); }
DI float bf_hi(u32 v) { return __uint_as_float(v & 0xffff0000u); }
DI u32 pack2(float lo, float hi) { f32x2 v = {lo, hi}; return __builtin_bit_cast(u32, __builtin_convertvector(v, bf16x2)); }
DI f32x4 mfma16(u32x4 a, u32x4 b, f32x4 c) {
  return __builtin_amdgcn_mfma_f32_16x16x32_bf16(__builtin_bit_cast(bf16x8, a), __builtin_bit_cast(bf16x8, b), c, 0, 0, 0);
}
#define ST_AUX 16
#define LD_AUX 16
#define LD_POL "sc1"
DI void glds16(const void* g, u32 lds_base) {
  u32 sv;
  asm volatile("s_mov_b32 %0, m0\n\ts_mov_b32 m0, %2\n\ts_nop 0\n\tglobal_load_lds_dwordx4 %1, off\n\ts_mov_b32 m0, %0"
               : "=&s"(sv) : "v"(g), "s"(lds_base) : "memory");
}
DI void glds16_sc1(const void* g, u32 lds_base) {
  u32 sv;
  asm volatile("s_mov_b32 %0, m0\n\ts_mov_b32 m0, %2\n\ts_nop 0\n\tglobal_load_lds_dwordx4 %1, off " LD_POL "\n\ts_mov_b32 m0, %0"
               : "=&s"(sv) : "v"(g), "s"(lds_base) : "memory");
}
DI u32x4 lds_r128(u32 a) { return *(const LDS3 u32x4*)(uintptr_t)a; }
DI u32x2 lds_r64(u32 a) { return *(const LDS3 u32x2*)(uintptr_t)a; }
DI u32x2 lds_r64v(u32 a) { return *(const volatile LDS3 u32x2*)(uintptr_t)a; }
DI void lds_w128(u32 a, u32x4 v) { *(LDS3 u32x4*)(uintptr_t)a = v; }
DI u32x2 lds_tr(u32 a) { return __builtin_bit_cast(u32x2, __builtin_amdgcn_ds_read_tr16_b64_v4i16((LDS3 s16x4*)(uintptr_t)a)); }
DI void st128_sc1(rsrc_t r, u32 off, u32x4 v) { __builtin_amdgcn_raw_buffer_store_b128(v, r, off, 0, ST_AUX); }
DI void st64_sc1(rsrc_t r, u32 off, u32x2 v) { __builtin_amdgcn_raw_buffer_store_b64(v, r, off, 0, 16); }
DI u32x4 ld128_sc1(rsrc_t r, u32 off) { return __builtin_amdgcn_raw_buffer_load_b128(r, off, 0, LD_AUX); }
DI u32x2 ld64_sc1(rsrc_t r, u32 off) { return __builtin_amdgcn_raw_buffer_load_b64(r, off, 0, 16); }
#define VM_BAR(N) asm volatile("s_waitcnt vmcnt(" #N ") lgkmcnt(0)\n\ts_barrier" ::: "memory")
#define LG_BAR() asm volatile("s_waitcnt lgkmcnt(0)\n\ts_barrier" ::: "memory")
#define VM0() asm volatile("s_waitcnt vmcnt(0)" ::: "memory")

DI u32 cnt_add(u32 lds0, int c) {
  asm volatile("s_waitcnt lgkmcnt(0)" ::: "memory");
  u32 old = 0;
  if ((threadIdx.x & 63) == 0) old = __hip_atomic_fetch_add((LDS3 u32*)(uintptr_t)(lds0 + L_CNT + 4 * c), 1u, __ATOMIC_RELAXED, __HIP_MEMORY_SCOPE_WORKGROUP);
  return __builtin_amdgcn_readfirstlane(old);
}
DI void cnt_sig(u32 lds0, int c) {
  asm volatile("s_waitcnt lgkmcnt(0)" ::: "memory");
  if ((threadIdx.x & 63) == 0) (void)__hip_atomic_fetch_add((LDS3 u32*)(uintptr_t)(lds0 + L_CNT + 4 * c), 1u, __ATOMIC_RELAXED, __HIP_MEMORY_SCOPE_WORKGROUP);
  asm volatile("" ::: "memory");
}
template <int SLP = 1>
DI void cnt_wait(u32 lds0, int c, u32 n) {
  _Pragma("unroll 1") for (u32 spins = 0; spins < SPIN_CAP; ++spins) {
    if (__builtin_amdgcn_readfirstlane(__hip_atomic_load((LDS3 u32*)(uintptr_t)(lds0 + L_CNT + 4 * c), __ATOMIC_RELAXED, __HIP_MEMORY_SCOPE_WORKGROUP)) >= n) break;
    __builtin_amdgcn_s_sleep(SLP);
  }
  asm volatile("" ::: "memory");
}
DI void flag_publish(rsrc_t r, u32 off, u32x2 tag) { if ((threadIdx.x & 63) == 0) st64_sc1(r, off, tag); }
DI bool flags_ok(u32x2 v, u32x2 tag, bool need) { return __all(!need || (v[0] == tag[0] && v[1] == tag[1])); }
DI void flags_wait(rsrc_t r, u32 off, bool need, u32x2 tag) {
  _Pragma("unroll 1") for (u32 spins = 0; spins < SPIN_CAP; ++spins) {
    asm volatile("" ::: "memory");
    const u32x2 v = ld64_sc1(r, off);
    if (flags_ok(v, tag, need)) break;
    __builtin_amdgcn_s_sleep(POLL_SLEEP);
  }
  asm volatile("" ::: "memory");
}
#define OUT_AUX 2
DI void out128(rsrc_t r, u32 off, u32x4 v) { __builtin_amdgcn_raw_buffer_store_b128(v, r, off, 0, OUT_AUX); }
DI void out64(rsrc_t r, u32 off, u32x2 v) { __builtin_amdgcn_raw_buffer_store_b64(v, r, off, 0, OUT_AUX); }
DI u32x4 swap_pack(u32 a0, u32 a1, u32 b0, u32 b1) {
  auto s0 = __builtin_amdgcn_permlane16_swap(a0, b0, false, false);
  auto s1 = __builtin_amdgcn_permlane16_swap(a1, b1, false, false);
  return u32x4{s0[0], s1[0], s0[1], s1[1]};
}
DI float xmax4(float m) {
  auto r = __builtin_amdgcn_permlane32_swap(__float_as_uint(m), __float_as_uint(m), false, false);
  m = fmaxf(__uint_as_float(r[0]), __uint_as_float(r[1]));
  auto s = __builtin_amdgcn_permlane16_swap(__float_as_uint(m), __float_as_uint(m), false, false);
  return fmaxf(__uint_as_float(s[0]), __uint_as_float(s[1]));
}
DI float ex2(float v) { return __builtin_amdgcn_exp2f(v); }

template <bool SWAP>
DI void gemm1_compute(u32 lds0, rsrc_t wr, int wid, int lane, int rt, int ct, u32 f1off, u32x2 tag) {
  asm volatile("" : "+v"(lane));
  const int l15 = lane & 15, g = lane >> 4, wm = wid >> 2, wn = wid & 3;
  const u32 xa0 = lds0 + (wm * 64 + l15) * 128 + ((g ^ (lane & 7)) * 16), xa1 = xa0 ^ 64;
  u32 wa[2][2];
  {
    const int q = l15 >> 2, p = lane & 3;
    UNR for (int nt = 0; nt < 2; ++nt) UNR for (int t = 0; t < 2; ++t) {
      const int swz = (q << 2) | ((2 * g + t) & 3);
      wa[nt][t] = lds0 + 16384 + 256 * (8 * g + 4 * t + q) + 16 * ((2 * (wn * 2 + nt) + (p >> 1)) ^ swz) + 8 * (p & 1);
    }
  }
  f32x4 acc[4][2];
  UNR for (int i = 0; i < 4; ++i) UNR for (int j = 0; j < 2; ++j) acc[i][j] = f32x4{0.f, 0.f, 0.f, 0.f};
  u32x4 xf[2][4], wf[2][2];
#define G1_RW(set, kt, ks, nt) do { const u32 sb_ = ((kt) % 3) * L_STAGE; \
    const u32x2 lo = lds_tr(wa[nt][0] + sb_ + (ks) * 8192), hi = lds_tr(wa[nt][1] + sb_ + (ks) * 8192); wf[set][nt] = u32x4{lo[0], lo[1], hi[0], hi[1]}; } while (0)
#define G1_RX(set, kt, ks, mt) xf[set][mt] = lds_r128(((ks) ? xa1 : xa0) + ((kt) % 3) * L_STAGE + (mt) * 2048)
#define G1_M(set, mt, nt) acc[mt][nt] = SWAP ? mfma16(wf[set][nt], xf[set][mt], acc[mt][nt]) : mfma16(xf[set][mt], wf[set][nt], acc[mt][nt])
#define G1_HALF(cs, ns, kt, ks, RD) do { \
    G1_M(cs, 0, 0); if (RD) G1_RW(ns, kt, ks, 0); SBAR(); \
    G1_M(cs, 0, 1); if (RD) { G1_RX(ns, kt, ks, 0); G1_RX(ns, kt, ks, 1); } SBAR(); \
    G1_M(cs, 1, 0); if (RD) G1_RW(ns, kt, ks, 1); SBAR(); \
    G1_M(cs, 1, 1); if (RD) G1_RX(ns, kt, ks, 2); SBAR(); \
    G1_M(cs, 2, 0); if (RD) G1_RX(ns, kt, ks, 3); SBAR(); \
    G1_M(cs, 2, 1); G1_M(cs, 3, 0); G1_M(cs, 3, 1); SBAR(); } while (0)
  G1_RW(0, 0, 0, 0); G1_RX(0, 0, 0, 0); G1_RW(0, 0, 0, 1); G1_RX(0, 0, 0, 1); G1_RX(0, 0, 0, 2); G1_RX(0, 0, 0, 3);
  SBAR();
  UNR for (int t = 0; t < 10; ++t) {
    G1_HALF(0, 1, t, 1, true);
    if (t < 9) {
      LG_BAR();
      SBAR();
    }
    G1_HALF(1, 0, t + 1, 0, t < 9);
  }
  if (SWAP) {
    const float sc = ct < 5 ? QSCALE : 1.0f;
    const u32 base = ct < 5 ? WS_Q : WS_K;
    UNR for (int nt = 0; nt < 2; ++nt) {
      const int G8 = ((ct % 5) * 128 + wn * 32 + nt * 16) / 8 + (g >> 1);
      const int pr = G8 / 5, gr = G8 - pr * 5;
      UNR for (int mp = 0; mp < 2; ++mp) {
        const f32x4 a = acc[2 * mp][nt], b = acc[2 * mp + 1][nt];
        const u32x4 v = swap_pack(pack2(a[0] * sc, a[1] * sc), pack2(a[2] * sc, a[3] * sc), pack2(b[0] * sc, b[1] * sc), pack2(b[2] * sc, b[3] * sc));
        const int tile = rt * 8 + wm * 4 + 2 * mp + (g & 1);
        st128_sc1(wr, base + ((pr * 128 + tile) * 5 + gr) * 256 + l15 * 16, v);
      }
    }
  } else {
    UNR for (int nt = 0; nt < 2; ++nt) {
      const int c = (ct - 10) * 128 + wn * 32 + nt * 16 + l15;
      const int h = c / 20, d = c - h * 20;
      const int off = d < 16 ? g * 256 + d * 16 : 1024 + g * 64 + (d - 16) * 16;
      UNR for (int mp = 0; mp < 2; ++mp) {
        const int blk = rt * 4 + wm * 2 + mp;
        const f32x4 a = acc[2 * mp][nt], b = acc[2 * mp + 1][nt];
        st128_sc1(wr, WS_V + (h * 64 + blk) * 1280 + off, u32x4{pack2(a[0], a[1]), pack2(a[2], a[3]), pack2(b[0], b[1]), pack2(b[2], b[3])});
      }
    }
  }
}

struct AttnOut { f32x4 oA0, oA1, oB0, oB1; };

DI void attn_slow(u32 ka, u32 va0, u32 va1, u32 v1inc, u32x4 qA, u32x4 qB, int kb0, int l15, int g, AttnOut& o) {
  const f32x4 z = f32x4{0.f, 0.f, 0.f, 0.f};
  float tA = -INFINITY, tB = -INFINITY;
  for (int t = 2 * kb0; t < 18; ++t) {
    const u32x4 kf = lds_r128(ka + t * 768);
    const f32x4 sA = mfma16(kf, qA, z), sB = mfma16(kf, qB, z);
    UNR for (int r = 0; r < 4; ++r) {
      const int dA = 16 * t + 4 * g + r - l15;
      tA = fmaxf(tA, (dA >= 1 && dA <= 256) ? sA[r] : -INFINITY);
      tB = fmaxf(tB, (dA >= 17 && dA <= 272) ? sB[r] : -INFINITY);
    }
  }
  const float mA = xmax4(tA), mB = xmax4(tB);
  o.oA0 = o.oA1 = o.oB0 = o.oB1 = z;
  for (int kbk = kb0; kbk < 9; ++kbk) {
    float pa[8], pb[8];
    UNR for (int hf = 0; hf < 2; ++hf) {
      const int t = 2 * kbk + hf;
      const u32x4 kf = lds_r128(ka + t * 768);
      const f32x4 sA = mfma16(kf, qA, z), sB = mfma16(kf, qB, z);
      UNR for (int r = 0; r < 4; ++r) {
        const int dA = 16 * t + 4 * g + r - l15;
        pa[4 * hf + r] = (dA >= 1 && dA <= 256) ? ex2(sA[r] - mA) : 0.f;
        pb[4 * hf + r] = (dA >= 17 && dA <= 272) ? ex2(sB[r] - mB) : 0.f;
      }
    }
    const u32x4 vf0 = lds_r128(va0 + kbk * 1280), vf1 = lds_r128(va1 + kbk * v1inc);
    const u32x4 pA = u32x4{pack2(pa[0], pa[1]), pack2(pa[2], pa[3]), pack2(pa[4], pa[5]), pack2(pa[6], pa[7])};
    const u32x4 pB = u32x4{pack2(pb[0], pb[1]), pack2(pb[2], pb[3]), pack2(pb[4], pb[5]), pack2(pb[6], pb[7])};
    o.oA0 = mfma16(vf0, pA, o.oA0); o.oA1 = mfma16(vf1, pA, o.oA1);
    o.oB0 = mfma16(vf0, pB, o.oB0); o.oB1 = mfma16(vf1, pB, o.oB1);
  }
}

DI void attn_fast(u32 ka, u32 va0, u32 va1, u32 v1inc, u32x4 qA, u32x4 qB, int l15, int g, AttnOut& o) {
  const f32x4 z = f32x4{0.f, 0.f, 0.f, 0.f};
  bool lo[4];
  UNR for (int r = 0; r < 4; ++r) lo[r] = 4 * g + r <= l15;
  u32x4 kf[2][2], vf[2][2], pA, pB;
  f32x4 S[2][4];
  kf[1][0] = lds_r128(ka + 16 * 768); kf[1][1] = lds_r128(ka + 17 * 768);
  kf[0][0] = lds_r128(ka); kf[0][1] = lds_r128(ka + 768);
  vf[1][0] = lds_r128(va0 + 8 * 1280); vf[1][1] = lds_r128(va1 + 8 * v1inc);
  f32x4 cA, cB;
  {
    const f32x4 sA = mfma16(kf[1][0], qA, z), sB = mfma16(kf[1][1], qB, z), sB0 = mfma16(kf[1][0], qB, z);
    SBAR();
    kf[1][0] = lds_r128(ka + 2 * 768); kf[1][1] = lds_r128(ka + 3 * 768);
    float mA = -INFINITY, mB = -INFINITY;
    UNR for (int r = 0; r < 4; ++r) { mA = fmaxf(mA, lo[r] ? sA[r] : -INFINITY); mB = fmaxf(mB, lo[r] ? sB[r] : -INFINITY); }
    mA = xmax4(mA); mB = xmax4(mB);
    cA = f32x4{-mA, -mA, -mA, -mA}; cB = f32x4{-mB, -mB, -mB, -mB};
    S[0][0] = mfma16(kf[0][0], qA, cA); S[0][1] = mfma16(kf[0][1], qA, cA); S[0][3] = mfma16(kf[0][1], qB, cB);
    float a[4], b0[4], b1[4];
    UNR for (int r = 0; r < 4; ++r) { a[r] = lo[r] ? ex2(sA[r] - mA) : 0.f; b0[r] = ex2(sB0[r] - mB); b1[r] = lo[r] ? ex2(sB[r] - mB) : 0.f; }
    pA = u32x4{pack2(a[0], a[1]), pack2(a[2], a[3]), 0u, 0u};
    pB = u32x4{pack2(b0[0], b0[1]), pack2(b0[2], b0[3]), pack2(b1[0], b1[1]), pack2(b1[2], b1[3])};
  }
  o.oA0 = o.oA1 = o.oB0 = o.oB1 = z;
  SBAR();
  UNR for (int k = 0; k < 8; ++k) {
    const int c = k & 1, n = c ^ 1;
    vf[c][0] = lds_r128(va0 + k * 1280); vf[c][1] = lds_r128(va1 + k * v1inc);
    if (k < 6) { kf[c][0] = lds_r128(ka + (2 * k + 4) * 768); kf[c][1] = lds_r128(ka + (2 * k + 5) * 768); }
    SBAR();
    u32 np[8];
    UNR for (int i = 0; i < 4; ++i) {
      if (k < 7) S[n][i] = mfma16(kf[n][i & 1], i < 2 ? qA : qB, i < 2 ? cA : cB);
      float e[4];
      if (k == 0 && i == 2) { np[4] = 0u; np[5] = 0u; }
      else {
        e[0] = ex2(S[c][i][0]); e[1] = ex2(S[c][i][1]);
      }
      if (i == 0) o.oA0 = mfma16(vf[n][0], pA, o.oA0);
      if (i == 1) o.oA1 = mfma16(vf[n][1], pA, o.oA1);
      if (i == 2) o.oB0 = mfma16(vf[n][0], pB, o.oB0);
      if (i == 3) o.oB1 = mfma16(vf[n][1], pB, o.oB1);
      if (!(k == 0 && i == 2)) {
        e[2] = ex2(S[c][i][2]); e[3] = ex2(S[c][i][3]);
        if (k == 0 && (i == 0 || i == 3)) { UNR for (int r = 0; r < 4; ++r) e[r] = lo[r] ? 0.f : e[r]; }
        np[2 * i] = pack2(e[0], e[1]); np[2 * i + 1] = pack2(e[2], e[3]);
      }
      SBAR();
    }
    pA = u32x4{np[0], np[1], np[2], np[3]}; pB = u32x4{np[4], np[5], np[6], np[7]};
  }
  o.oA0 = mfma16(vf[1][0], pA, o.oA0); o.oA1 = mfma16(vf[1][1], pA, o.oA1);
  o.oB0 = mfma16(vf[1][0], pB, o.oB0); o.oB1 = mfma16(vf[1][1], pB, o.oB1);
}

template <int TMASK, int NR>
DI void conv_accum(u32 ha, u32 xa, f32x4 (&acc)[8]) {
  constexpr int NF = (TMASK & 0xC0) ? 8 : 6;
  u32x4 A[8], B[8];
#define CV_LA(i, rr) do { if ((i) < NF) { const u32x2 lo_ = lds_r64v(ha + (rr) * 64 + (i) * 512), hi_ = lds_r64v(ha + (rr) * 64 + (i) * 512 + 8); \
    A[i] = u32x4{lo_[0], lo_[1], hi_[0], hi_[1]}; } } while (0)
#define CV_LB(m, rr) do { if ((m) < NF) B[m] = lds_r128(xa + (rr) * 64 + (7 - (m)) * 512); } while (0)
#define CV_MM(i, m) do { if ((i) < NF && (m) < NF && (i) + (m) < 8 && ((TMASK >> ((i) + (m))) & 1)) acc[(i) + (m)] = mfma16(A[i], B[m], acc[(i) + (m)]); } while (0)
  CV_LB(0, 0); UNR for (int i = 7; i >= 4; --i) CV_LA(i, 0);
  UNR for (int m = 1; m < 4; ++m) CV_LB(m, 0);
  UNR for (int i = 0; i < 4; ++i) { CV_LA(i, 0); CV_LB(7 - i, 0); }
  SBAR();
  UNR for (int rr = 0; rr < NR; ++rr) {
    UNR for (int m = 0; m < 4; ++m) UNR for (int i = 7; i >= 4; --i) CV_MM(i, m);
    SBAR();
    if (rr + 1 < NR) { UNR for (int i = 7; i >= 4; --i) CV_LA(i, rr + 1); }
    SBAR();
    UNR for (int i = 0; i < 4; ++i) UNR for (int m = 7; m >= 4; --m) CV_MM(i, m);
    SBAR();
    if (rr + 1 < NR) { UNR for (int m = 7; m >= 4; --m) CV_LB(m, rr + 1); }
    SBAR();
    UNR for (int m = 0; m < 4; ++m) {
      UNR for (int i = 0; i < 4; ++i) CV_MM(i, m);
      SBAR();
      if (rr + 1 < NR) CV_LB(m, rr + 1);
      SBAR();
    }
    if (rr + 1 < NR) { UNR for (int i = 0; i < 4; ++i) CV_LA(i, rr + 1); }
    SBAR();
  }
}
DI u32x2 conv_fin(f32x4 a, int n, u32 xbase, float dcoef, int l15, int g) {
  const int s = 256 * n + 16 * l15 + 4 * g;
  const u32x2 xv = lds_r64(xbase + (2044 - s) * 2);
  const u32 y01 = pack2(a[0], a[1]), y23 = pack2(a[2], a[3]);
  const u32 d01 = pack2(bf_hi(xv[1]) * dcoef, bf_lo(xv[1]) * dcoef), d23 = pack2(bf_hi(xv[0]) * dcoef, bf_lo(xv[0]) * dcoef);
  return u32x2{pack2(bf_lo(y01) + bf_lo(d01), bf_hi(y01) + bf_hi(d01)), pack2(bf_lo(y23) + bf_lo(d23), bf_hi(y23) + bf_hi(d23))};
}
DI void conv_store2(rsrc_t wr, u32 rowoff, u32x2 ya, int na, u32x2 yb, int nb, int l15, int g) {
  const u32x4 v = swap_pack(ya[0], ya[1], yb[0], yb[1]);
  st128_sc1(wr, rowoff + (256 * ((g & 1) ? nb : na) + 16 * l15 + 8 * (g >> 1)) * 2, v);
}
DI void conv_store1(rsrc_t wr, u32 rowoff, u32x2 ya, int na, int l15, int g) { st64_sc1(wr, rowoff + (256 * na + 16 * l15 + 4 * g) * 2, ya); }

DI u32 kv_pair(u32 k0, u32 k1, u32 k2, u32 k3, u32 v, u32 sa01, u32 sa23, u32 sb01, u32 sb23) {
  u32 p = pack2(bf_lo(k0) * bf_hi(sa23), bf_hi(k0) * bf_hi(sb23));
  u32 q = pack2(bf_lo(k1) * bf_lo(sa23), bf_hi(k1) * bf_lo(sb23));
  p = pack2(bf_lo(p) + bf_lo(q), bf_hi(p) + bf_hi(q));
  q = pack2(bf_lo(k2) * bf_hi(sa01), bf_hi(k2) * bf_hi(sb01));
  p = pack2(bf_lo(p) + bf_lo(q), bf_hi(p) + bf_hi(q));
  q = pack2(bf_lo(k3) * bf_lo(sa01), bf_hi(k3) * bf_lo(sb01));
  p = pack2(bf_lo(p) + bf_lo(q), bf_hi(p) + bf_hi(q));
  return pack2(bf_lo(p) * bf_lo(v), bf_hi(p) * bf_hi(v));
}
DI u32 lo2(u32 a, u32 b) { return __builtin_amdgcn_perm(b, a, 0x05040100u); }
DI u32 hi2(u32 a, u32 b) { return __builtin_amdgcn_perm(b, a, 0x07060302u); }

__global__ __launch_bounds__(1024, 4) void fused_kernel(const u16* __restrict__ qin, const u16* __restrict__ kin, const u16* __restrict__ vin,
                                                         const u16* __restrict__ shk, const u16* __restrict__ ssmk, const u16* __restrict__ ssmD,
                                                         const u16* __restrict__ x, const u16* __restrict__ wqkv, const u16* __restrict__ wo,
                                                         char* __restrict__ ws, u16* __restrict__ out) {
  __shared__ __attribute__((aligned(1024))) char lds[LDS_BYTES];
  const u32 lds0 = (u32)(uintptr_t)lds;
  int tid = threadIdx.x, lane = tid & 63;
  const int wid = __builtin_amdgcn_readfirstlane(tid >> 6);
  int l15 = lane & 15, g = lane >> 4;
#define FRESH_IDS() do { asm volatile("" : "+v"(tid)); lane = tid & 63; l15 = lane & 15; g = lane >> 4; } while (0)
  const int b = blockIdx.x, xcd = b & 7, idx = b >> 3;
  const rsrc_t wr = __builtin_amdgcn_make_buffer_rsrc(ws, 0, WS_END, 0x00020000);
  const rsrc_t orr = __builtin_amdgcn_make_buffer_rsrc(out, 0, 2048 * 1280 * 2, 0x00020000);
  STAMP_DECL; STAMP(0);
  u32 code_touch = 0;
  if (wid == 15) {
    u64 pc; u32 len;
    asm volatile("s_getpc_b64 %0\n.Lpf_here:\n\ts_mov_b32 %1, .Lpf_end-.Lpf_here" : "=s"(pc), "=s"(len));
    const u32 off = (u32)(idx * 16 + (lane & 15)) * 128u;
    const __attribute__((address_space(1))) u32* cp = (const __attribute__((address_space(1))) u32*)(pc + (off + 4 <= len ? off : 0u));
    code_touch = *cp;
  }
  u32x2 tag;
  const int jr = xcd >> 1, jc = xcd & 1;
  const bool has_gemm = !(jc == 1 && idx >= 28);
  const int rt = jr * 4 + (idx & 3), ct = jc * 8 + (idx >> 2);
  const int cbase = 80 * xcd + 5 * (idx >> 1), odd = idx & 1;
  const int ch0 = cbase + (odd ? 3 : 0), ch2 = cbase + 2;

  if (wid < 8) {
    {
      const int it = tid < 320 ? tid : tid - 192;
      const int cq = it % 20, rg = it / 20;
      const int s0 = idx * 64 + rg * 4, c0 = xcd * 80 + cq * 4;
      u32x2 kk[7], vv[4];
      u32x4 sh0, sh1;
      if (wid < 5) {
        UNR for (int j = 0; j < 7; ++j) { const int r = s0 - 3 + j; kk[j] = __builtin_nontemporal_load((const u32x2*)(kin + (size_t)(r < 0 ? 0 : r) * 640 + c0)); }
        UNR for (int j = 0; j < 4; ++j) vv[j] = __builtin_nontemporal_load((const u32x2*)(vin + (size_t)(s0 + j) * 640 + c0));
        sh0 = *(const u32x4*)(shk + c0 * 4); sh1 = *(const u32x4*)(shk + c0 * 4 + 8);
      }
      const u32x2 ep = ld64_sc1(wr, WS_EPOCH);
      if (tid >= 320 && tid < 320 + C_NUM) *(LDS3 u32*)(uintptr_t)(lds0 + L_CNT + (tid - 320) * 4) = 0u;
      if (tid >= 384 && tid < 480) lds_w128(lds0 + L_XR + ((tid - 384) >> 5) * XR_CH + 4096 + (tid & 31) * 16, u32x4{0u, 0u, 0u, 0u});
      LG_BAR();
      if (wid < 5) {
        if (s0 == 0) { kk[0] = u32x2{0u, 0u}; kk[1] = u32x2{0u, 0u}; kk[2] = u32x2{0u, 0u}; }
        u32 r01[4], r23[4];
        UNR for (int j = 0; j < 4; ++j) {
          r01[j] = kv_pair(kk[j][0], kk[j + 1][0], kk[j + 2][0], kk[j + 3][0], vv[j][0], sh0[0], sh0[1], sh0[2], sh0[3]);
          r23[j] = kv_pair(kk[j][1], kk[j + 1][1], kk[j + 2][1], kk[j + 3][1], vv[j][1], sh1[0], sh1[1], sh1[2], sh1[3]);
        }
        const u32 a = lds0 + L_KV + (cq * 4) * 144 + (60 - 4 * rg) * 2;
        *(LDS3 u32x2*)(uintptr_t)(a) = u32x2{lo2(r01[3], r01[2]), lo2(r01[1], r01[0])};
        *(LDS3 u32x2*)(uintptr_t)(a + 144) = u32x2{hi2(r01[3], r01[2]), hi2(r01[1], r01[0])};
        *(LDS3 u32x2*)(uintptr_t)(a + 288) = u32x2{lo2(r23[3], r23[2]), lo2(r23[1], r23[0])};
        *(LDS3 u32x2*)(uintptr_t)(a + 432) = u32x2{hi2(r23[3], r23[2]), hi2(r23[1], r23[0])};
      }
      const u64 e = (((u64)ep[1] << 32) | ep[0]) + 1;
      tag = u32x2{(u32)__builtin_amdgcn_readfirstlane((u32)e), (u32)__builtin_amdgcn_readfirstlane((u32)(e >> 32))};
      if (tid == 448) *(LDS3 u32x2*)(uintptr_t)(lds0 + L_TAG) = tag;
      STAMP(11);
      cnt_sig(lds0, C_S1); cnt_wait(lds0, C_S1, 8);
      STAMP(12);
      st128_sc1(wr, WS_XR + (xcd * 80 + (tid >> 3)) * 4096 + (1984 - 64 * idx) * 2 + (tid & 7) * 16, lds_r128(lds0 + L_KV + (tid >> 3) * 144 + (tid & 7) * 16));
      if (tid < 128) st128_sc1(wr, WS_XR + (xcd * 80 + 64 + (tid >> 3)) * 4096 + (1984 - 64 * idx) * 2 + (tid & 7) * 16,
                               lds_r128(lds0 + L_KV + (64 + (tid >> 3)) * 144 + (tid & 7) * 16));
      VM0();
      if (cnt_add(lds0, C_H) == 7) flag_publish(wr, WS_F1 + (xcd * 32 + idx) * FSTRIDE, tag);
      LG_BAR();
      STAMP(13);
      FRESH_IDS();
      if (has_gemm) {
        if (ct < 10) gemm1_compute<true>(lds0, wr, wid, lane, rt, ct, WS_F1 + (xcd * 32 + idx) * FSTRIDE, tag);
        else gemm1_compute<false>(lds0, wr, wid, lane, rt, ct, WS_F1 + (xcd * 32 + idx) * FSTRIDE, tag);
      } else {
        UNR for (int t = 1; t < 10; ++t) LG_BAR();
      }
    }
    STAMP(1);
    VM0();
    if (cnt_add(lds0, C_B) == 7 && has_gemm) flag_publish(wr, WS_F2 + (rt * 15 + ct) * FSTRIDE, tag);
    STAMP(2);
    FRESH_IDS();
    const int head = xcd * 4 + (idx >> 3), qb = idx & 7, pr = head >> 1, g0 = 2 * (head & 1);
    if (wid == 0) {
      const int which = lane >> 3, r = (lane >> 1) & 3, hi = lane & 1;
      const int prt = 2 * qb - 2 + r, pct = 5 * which + ((20 * head + (hi ? 19 : 0)) >> 7);
      const bool need = lane < 24 && prt >= 0 && !(which == 0 && r < 2);
      flags_wait(wr, WS_F2 + (need ? (prt * 15 + pct) * FSTRIDE : 0), need, tag);
      cnt_add(lds0, C_QKV);
    }
    cnt_wait(lds0, C_B, 8);
    if (qb == 0) {
      lds_w128(lds0 + L_AK + tid * 16, u32x4{0u, 0u, 0u, 0u});
      if (tid < 256) lds_w128(lds0 + L_AK + 8192 + tid * 16, u32x4{0u, 0u, 0u, 0u});
      lds_w128(lds0 + L_AV + tid * 16, u32x4{0u, 0u, 0u, 0u});
      if (tid < 128) lds_w128(lds0 + L_AV + 8192 + tid * 16, u32x4{0u, 0u, 0u, 0u});
    }
    if (tid < 16) { const u32 o1 = (qb > 0 || tid >= 8) ? 0x3f803f80u : 0u; lds_w128(lds0 + L_ONES + tid * 16, u32x4{o1, o1, o1, o1}); }
    const char* ksrc[3];
    UNR for (int j = 0; j < 3; ++j) {
      const int s = 64 * j + lane, i = s / 48, u = s - i * 48;
      ksrc[j] = ws + WS_K + (((pr * 128 + qb * 16 - 16 + 4 * wid + i) * 5 + g0) * 256 + u * 16);
      asm volatile("" : "+v"(ksrc[j]));
    }
    const char* vsrc = ws + WS_V + ((head * 64 + qb * 8 - 8) * 1280 + wid * 1024 + lane * 16);
    asm volatile("" : "+v"(vsrc));
    cnt_wait<2>(lds0, C_QKV, 1);
    STAMP(3);
    if (qb > 0 || wid >= 4) { UNR for (int j = 0; j < 3; ++j) glds16_sc1(ksrc[j], lds0 + L_AK + (3 * wid + j) * 1024); }
    UNR for (int j = 0; j < 3; ++j) {
      const int n = wid + 8 * j;
      if (n < 20 && (qb > 0 || n >= 10)) glds16_sc1(vsrc + j * 8192, lds0 + L_AV + n * 1024);
    }
    const int qt = qb * 16 + wid * 2, gq = g < 3 ? g : 2;
    u32x4 qA = ld128_sc1(wr, WS_Q + ((pr * 128 + qt) * 5 + g0 + gq) * 256 + l15 * 16);
    u32x4 qB = ld128_sc1(wr, WS_Q + ((pr * 128 + qt + 1) * 5 + g0 + gq) * 256 + l15 * 16);
    {
      const bool zlo = g == 3 || ((head & 1) && g == 0), zhi = g == 3 || (!(head & 1) && g == 2);
      if (zlo) { qA[0] = 0u; qA[1] = 0u; qB[0] = 0u; qB[1] = 0u; }
      if (zhi) { qA[2] = 0u; qA[3] = 0u; qB[2] = 0u; qB[3] = 0u; }
    }
    VM0();
    cnt_add(lds0, C_KV);
    cnt_wait(lds0, C_KV, 8);
    STAMP(4);
    u32 tw;
    {
      const char* p1 = (const char*)wo + (size_t)(idx * 200 + (tid < 200 ? tid : 0)) * 128;
      const char* p2 = (const char*)qin + (size_t)(idx * 64 + ((tid & 127) >> 1)) * 1280 + xcd * 160 + (tid & 1) * 128;
      tw = *(const u32*)(tid < 256 ? p1 : p2);
    }
    {
      const u32 ka = lds0 + L_AK + wid * 1536 + gq * 256 + l15 * 16;
      const u32 va0 = lds0 + L_AV + wid * 1280 + g * 256 + l15 * 16;
      const u32 va1 = l15 < 4 ? lds0 + L_AV + wid * 1280 + 1024 + g * 64 + l15 * 16 : lds0 + L_ONES + wid * 16;
      const u32 v1inc = l15 < 4 ? 1280u : 16u;
      AttnOut o;
      attn_fast(ka, va0, va1, v1inc, qA, qB, l15, g, o);
      float lA = __shfl(o.oA1[0], 16 + l15), lB = __shfl(o.oB1[0], 16 + l15);
      if (__any(!(lA <= 16777216.f) || !(lB <= 16777216.f))) {
        attn_slow(ka, va0, va1, v1inc, qA, qB, qb == 0 ? 8 - wid : 0, l15, g, o);
        lA = __shfl(o.oA1[0], 16 + l15); lB = __shfl(o.oB1[0], 16 + l15);
      }
      const float iA = 1.0f / lA, iB = 1.0f / lB;
      const u32x4 v = swap_pack(pack2(o.oA0[0] * iA, o.oA0[1] * iA), pack2(o.oA0[2] * iA, o.oA0[3] * iA),
                                pack2(o.oB0[0] * iB, o.oB0[1] * iB), pack2(o.oB0[2] * iB, o.oB0[3] * iB));
      st128_sc1(wr, WS_O + (qt + (g & 1)) * 20480 + (5 * pr + g0 + (g >> 1)) * 256 + l15 * 16, v);
      const u32x4 w = swap_pack(pack2(o.oA1[0] * iA, o.oA1[1] * iA), pack2(o.oA1[2] * iA, o.oA1[3] * iA),
                                pack2(o.oB1[0] * iB, o.oB1[1] * iB), pack2(o.oB1[2] * iB, o.oB1[3] * iB));
      if (g < 2) st64_sc1(wr, WS_O + (qt + g) * 20480 + (5 * pr + 4) * 256 + l15 * 16 + (head & 1) * 8, u32x2{w[0], w[1]});
    }
    STAMP(5);
    VM0();
    asm volatile("" :: "v"(tw));
    if (cnt_add(lds0, C_O) == 7) flag_publish(wr, WS_F4 + (qb * 32 + head) * FSTRIDE, tag);
    STAMP(6);
    FRESH_IDS();
    const int rb = xcd * 4 + (idx & 3), cb = idx >> 2;
    if (wid == 0) {
      flags_wait(wr, WS_F4 + (xcd * 32 + (lane & 31)) * FSTRIDE, true, tag);
      cnt_add(lds0, C_OOK);
    }
    cnt_wait<2>(lds0, C_OOK, 1);
    STAMP(7);
    {
      const int mt = wid & 3, kh = wid >> 2;
      u32x4 of[10];
      UNR for (int ks = 0; ks < 10; ++ks) of[ks] = ld128_sc1(wr, WS_O + (rb * 4 + mt) * 20480 + (kh * 10 + ks) * 1024 + lane * 16);
      cnt_wait(lds0, C_W, 8);
      cnt_wait(lds0, C_O, 8);
      STAMP(8);
      const u32 wa = lds0 + L_WO + kh * 51200 + (16 * (g >> 1) + 4 * (g & 1) + (l15 >> 2)) * 160 + (lane & 3) * 8;
      f32x4 acc[5];
      UNR for (int j = 0; j < 5; ++j) acc[j] = f32x4{0.f, 0.f, 0.f, 0.f};
      UNR for (int ks = 0; ks < 10; ++ks) {
        UNR for (int j = 0; j < 5; ++j) {
          const u32x2 lo = lds_tr(wa + ks * 5120 + j * 32), hi = lds_tr(wa + ks * 5120 + 1280 + j * 32);
          acc[j] = mfma16(u32x4{lo[0], lo[1], hi[0], hi[1]}, of[ks], acc[j]);
        }
      }
      STAMP(9);
      const u32 red = lds0 + L_RED3 + mt * 5120 + lane * 16;
      if (kh == 0) { lds_w128(red + 2 * 1024, __builtin_bit_cast(u32x4, acc[2])); lds_w128(red + 3 * 1024, __builtin_bit_cast(u32x4, acc[3])); }
      else { lds_w128(red, __builtin_bit_cast(u32x4, acc[0])); lds_w128(red + 1024, __builtin_bit_cast(u32x4, acc[1])); lds_w128(red + 4 * 1024, __builtin_bit_cast(u32x4, acc[4])); }
      cnt_add(lds0, C_R0 + mt);
      cnt_wait(lds0, C_R0 + mt, 2);
      const u32 orow = ((rb * 64 + mt * 16 + l15) * 1280 + 640 + cb * 80) * 2;
      if (kh == 0) {
        const f32x4 a = acc[0] + __builtin_bit_cast(f32x4, lds_r128(red)), c = acc[1] + __builtin_bit_cast(f32x4, lds_r128(red + 1024));
        const f32x4 e = acc[4] + __builtin_bit_cast(f32x4, lds_r128(red + 4 * 1024));
        out128(orr, orow + ((g & 1) * 16 + (g >> 1) * 8) * 2, swap_pack(pack2(a[0], a[1]), pack2(a[2], a[3]), pack2(c[0], c[1]), pack2(c[2], c[3])));
        out64(orr, orow + (64 + 4 * g) * 2, u32x2{pack2(e[0], e[1]), pack2(e[2], e[3])});
      } else {
        const f32x4 a = acc[2] + __builtin_bit_cast(f32x4, lds_r128(red + 2 * 1024)), c = acc[3] + __builtin_bit_cast(f32x4, lds_r128(red + 3 * 1024));
        out128(orr, orow + (32 + (g & 1) * 16 + (g >> 1) * 8) * 2, swap_pack(pack2(a[0], a[1]), pack2(a[2], a[3]), pack2(c[0], c[1]), pack2(c[2], c[3])));
      }
    }
    STAMP(10);
  } else {
    {
      const int aw = wid - 8, t = tid - 512, tc = t & 255;
      u32x4 hd[2]; u32x2 he[2];
      UNR for (int c = 0; c < 2; ++c) {
        const u16* hp = ssmk + (size_t)(c == 0 ? ch0 + (t >> 8) : ch2) * 2048 + tc * 8;
        hd[c] = __builtin_nontemporal_load((const u32x4*)hp);
        he[c] = __builtin_nontemporal_load((const u32x2*)(hp + (tc < 255 ? 8 : 0)));
      }
#define H_WRITE(i, c) do { const u32x4 d = hd[i]; const u32x2 e = he[i]; \
        const u32 a01 = __builtin_amdgcn_alignbit(d[1], d[0], 16), a12 = __builtin_amdgcn_alignbit(d[2], d[1], 16), a23 = __builtin_amdgcn_alignbit(d[3], d[2], 16), \
                  a34 = __builtin_amdgcn_alignbit(e[0], d[3], 16), a45 = __builtin_amdgcn_alignbit(e[1], e[0], 16); \
        const u32 a = lds0 + L_HC + (c) * HC_CH + 32 + tc * 16; \
        lds_w128(a, d); lds_w128(a + HC_STRIDE, u32x4{a01, a12, a23, a34}); \
        lds_w128(a + 2 * HC_STRIDE, u32x4{d[1], d[2], d[3], e[0]}); lds_w128(a + 3 * HC_STRIDE, u32x4{a12, a23, a34, a45}); \
        if (tc == 0) { const u32 z = 0u; \
          lds_w128(a - 32, u32x4{z, z, z, z}); lds_w128(a - 16, u32x4{z, z, z, z}); \
          lds_w128(a - 32 + HC_STRIDE, u32x4{z, z, z, z}); lds_w128(a - 16 + HC_STRIDE, u32x4{z, z, z, d[0] << 16}); \
          lds_w128(a - 32 + 2 * HC_STRIDE, u32x4{z, z, z, z}); lds_w128(a - 16 + 2 * HC_STRIDE, u32x4{z, z, z, d[0]}); \
          lds_w128(a - 32 + 3 * HC_STRIDE, u32x4{z, z, z, z}); lds_w128(a - 16 + 3 * HC_STRIDE, u32x4{z, z, d[0] << 16, a01}); } } while (0)
#define H_PIN() asm volatile("" : "+v"(hd[0]), "+v"(hd[1]), "+v"(he[0]), "+v"(he[1]))
      u32 touch = 0;
      u32x2 f1 = u32x2{0u, 0u};
      const u32 f1o = WS_F1 + (xcd * 32 + (lane & 31)) * FSTRIDE;
      bool xr_issued = false;
#define GET_TAG() do { const u32x2 tg = lds_r64v(lds0 + L_TAG); tag = u32x2{(u32)__builtin_amdgcn_readfirstlane(tg[0]), (u32)__builtin_amdgcn_readfirstlane(tg[1])}; } while (0)
#define XR_ISSUE() do { UNR for (int i = 0; i < 2; ++i) { const int j = aw + 8 * i, c = j >> 2, p = j & 3; \
        if (j < 12) glds16_sc1(ws + WS_XR + (size_t)((c < 2 ? ch0 + c : ch2) * 4096 + p * 1024 + lane * 16), lds0 + L_XR + c * XR_CH + p * 1024); } } while (0)
      asm volatile("s_barrier" ::: "memory");
      {
        const int lid = idx * 512 + t;
        const char* tp;
        if (lid < 5120) tp = (const char*)x + (size_t)jr * 512 * 1280 + (size_t)lid * 128;
        else { const int l2 = lid - 5120; int row = l2 >> 4, c = l2 & 15; row = row > 639 ? 639 : row; c = (jc == 1 && c > 13) ? 13 : c;
               tp = (const char*)wqkv + (size_t)row * 3840 + jc * 2048 + c * 128; }
        touch = *(const u32*)tp;
      }
      SBAR();
      if (t < 256) { H_WRITE(0, 0); H_WRITE(1, 2); } else H_WRITE(0, 1);
      if (has_gemm) {
        const char* xs = (const char*)x + (size_t)(rt * 128 + aw * 16 + (lane >> 3)) * 1280 + (((lane & 7) ^ (lane >> 3)) * 16);
        const char* ws0 = (const char*)wqkv + (size_t)(aw * 8 + g) * 3840 + ct * 256 + ((l15 ^ ((g << 2) | ((2 * aw) & 3))) * 16);
        const char* ws1 = (const char*)wqkv + (size_t)(aw * 8 + 4 + g) * 3840 + ct * 256 + ((l15 ^ ((g << 2) | ((2 * aw + 1) & 3))) * 16);
        const u32 xd = lds0 + aw * 2048, wd = lds0 + 16384 + aw * 2048;
#define L_ISSUE(kt) do { const u32 sb_ = ((kt) % 3) * L_STAGE; \
          glds16(xs + (kt) * 128, xd + sb_); glds16(ws0 + (size_t)(kt) * 64 * 3840, wd + sb_); \
          glds16(xs + 8 * 1280 + (kt) * 128, xd + sb_ + 1024); glds16(ws1 + (size_t)(kt) * 64 * 3840, wd + sb_ + 1024); } while (0)
        L_ISSUE(0); L_ISSUE(1);
        STAMP(1);
        VM_BAR(4); STAMP(2); CSTAMP(13); L_ISSUE(2);
        VM_BAR(4); L_ISSUE(3);
        VM_BAR(4); L_ISSUE(4);
        VM_BAR(4); L_ISSUE(5);
        VM_BAR(4); STAMP(3); L_ISSUE(6);
        VM_BAR(4); L_ISSUE(7);
        if (wid == 8) {
          const char* f1p = ws + f1o;
          asm volatile("global_load_dwordx2 %0, %1, off sc1\n\ts_waitcnt vmcnt(5) lgkmcnt(0)\n\ts_barrier" : "=v"(f1) : "v"(f1p) : "memory");
          L_ISSUE(8);
          asm volatile("s_waitcnt vmcnt(4)" : "+v"(f1) :: "memory");
          GET_TAG();
          if (flags_ok(f1, tag, true)) cnt_sig(lds0, C_S2);
          LG_BAR();
        } else {
          VM_BAR(4); L_ISSUE(8);
          VM_BAR(4);
        }
        L_ISSUE(9);
        xr_issued = __builtin_amdgcn_readfirstlane(*(const volatile LDS3 u32*)(uintptr_t)(lds0 + L_CNT + 4 * C_S2)) != 0;
        if (xr_issued) {
          XR_ISSUE();
          if (aw < 4) { VM_BAR(6); VM_BAR(2); } else { VM_BAR(5); VM_BAR(1); }
        } else { VM_BAR(4); VM_BAR(0); }
        STAMP(12); CSTAMP(14);
      } else {
        UNR for (int i = 0; i < 10; ++i) LG_BAR();
      }
      asm volatile("" :: "v"(touch));
      GET_TAG();
      if (!xr_issued) {
        cnt_wait(lds0, C_B, 8);
        if (wid == 8) { flags_wait(wr, f1o, true, tag); cnt_sig(lds0, C_S2); }
        cnt_wait(lds0, C_S2, 1);
        XR_ISSUE();
      }
      VM0();
      cnt_sig(lds0, C_XR);
    }
    STAMP(4);
    FRESH_IDS();
    {
      const int cw = wid - 8;
      const int c = cw < 4 ? (cw >> 1) : 2;
      const int chn = c < 2 ? ch0 + c : ch2;
      const float dcoef = bf_lo((u32)ssmD[chn]);
      const u32 xb = lds0 + L_XR + c * XR_CH;
      const u32 ha = lds0 + L_HC + c * HC_CH + ((l15 + 1) & 3) * HC_STRIDE + (8 * g + ((l15 + 1) & ~3)) * 2;
      const u32 xa = xb + 480 - 32 * l15 + 16 * g;
      const u32 yrow = WS_Y2 + chn * 4096;
      f32x4 acc[8];
      UNR for (int i = 0; i < 8; ++i) acc[i] = f32x4{0.f, 0.f, 0.f, 0.f};
      cnt_wait(lds0, C_XR, 8);
      STAMP(5);
      if (cw < 4) {
        const int hf = cw & 1;
        conv_accum<0xFF, 4>(ha + hf * 256, xa + hf * 256, acc);
        STAMP(6);
        __builtin_amdgcn_s_setprio(1);
        const u32 red = lds0 + L_RED1 + c * 8192 + lane * 16;
        cnt_wait(lds0, C_B, 8);
        if (hf == 0) { UNR for (int i = 0; i < 4; ++i) lds_w128(red + i * 1024, __builtin_bit_cast(u32x4, acc[4 + i])); }
        else { UNR for (int i = 0; i < 4; ++i) lds_w128(red + 4096 + i * 1024, __builtin_bit_cast(u32x4, acc[i])); }
        cnt_add(lds0, C_P0 + c);
        cnt_wait(lds0, C_P0 + c, 2);
        u32x2 y[4];
        if (hf == 0) { UNR for (int i = 0; i < 4; ++i) y[i] = conv_fin(acc[i] + __builtin_bit_cast(f32x4, lds_r128(red + 4096 + i * 1024)), i, xb, dcoef, l15, g); }
        else { UNR for (int i = 0; i < 4; ++i) y[i] = conv_fin(acc[4 + i] + __builtin_bit_cast(f32x4, lds_r128(red + i * 1024)), 4 + i, xb, dcoef, l15, g); }
        conv_store2(wr, yrow, y[0], 4 * hf, y[1], 4 * hf + 1, l15, g);
        conv_store2(wr, yrow, y[2], 4 * hf + 2, y[3], 4 * hf + 3, l15, g);
      } else {
        const int qr = cw - 4;
        const u32 red = lds0 + L_RED2 + lane * 16;
        if (!odd) {
          conv_accum<0xC4, 2>(ha + qr * 128, xa + qr * 128, acc);
          STAMP(6);
          __builtin_amdgcn_s_setprio(1);
          cnt_wait(lds0, C_B, 8);
          const int tl[3] = {7, 6, 2};
          UNR for (int j = 0; j < 3; ++j) if (qr != j) lds_w128(red + (j * 3 + (qr > j ? qr - 1 : qr)) * 1024, __builtin_bit_cast(u32x4, acc[tl[j]]));
          cnt_add(lds0, C_Q4);
          cnt_wait(lds0, C_Q4, 4);
          UNR for (int j = 0; j < 3; ++j) if (qr == j) {
            f32x4 a = acc[tl[j]];
            UNR for (int w = 0; w < 3; ++w) a += __builtin_bit_cast(f32x4, lds_r128(red + (j * 3 + w) * 1024));
            conv_store1(wr, yrow, conv_fin(a, tl[j], xb, dcoef, l15, g), tl[j], l15, g);
          }
        } else {
          conv_accum<0x3B, 2>(ha + qr * 128, xa + qr * 128, acc);
          STAMP(6);
          __builtin_amdgcn_s_setprio(1);
          cnt_wait(lds0, C_B, 8);
          const int tl[5] = {5, 4, 3, 1, 0};
          UNR for (int j = 0; j < 5; ++j) { const int ow = j & 3; if (qr != ow) lds_w128(red + (j * 3 + (qr > ow ? qr - 1 : qr)) * 1024, __builtin_bit_cast(u32x4, acc[tl[j]])); }
          cnt_add(lds0, C_Q4);
          cnt_wait(lds0, C_Q4, 4);
          UNR for (int j = 0; j < 5; ++j) if (qr == (j & 3)) {
            f32x4 a = acc[tl[j]];
            UNR for (int w = 0; w < 3; ++w) a += __builtin_bit_cast(f32x4, lds_r128(red + (j * 3 + w) * 1024));
            conv_store1(wr, yrow, conv_fin(a, tl[j], xb, dcoef, l15, g), tl[j], l15, g);
          }
        }
      }
    }
    STAMP(7);
    __builtin_amdgcn_s_setprio(0);
    VM0();
    if (cnt_add(lds0, C_Y) == 7) flag_publish(wr, WS_F3 + (xcd * 32 + idx) * FSTRIDE, tag);
    cnt_wait<4>(lds0, C_Y, 8);
    STAMP(8);
    FRESH_IDS();
    cnt_wait<4>(lds0, C_KV, 8);
    __builtin_amdgcn_s_setprio(1);
    {
      const int aw = wid - 8, cb = idx >> 2;
      const int rg = lane / 10, ck = lane - rg * 10;
      UNR for (int i = 0; i < 4; ++i) {
        const int rho = (aw + 8 * i) * 5 + (lane < 50 ? rg : 0);
        const int r = rho & 31, G = 4 * (rho >> 5) + 2 * (r >> 4) + ((r >> 2) & 1), j = 4 * ((r >> 3) & 1) + (r & 3);
        const int ii = (G * 205) >> 10, u = G - ii * 5;
        const int krow = 40 * ii + (u < 4 ? 20 * (u >> 1) + 8 * (u & 1) + j : (j < 4 ? 16 + j : 32 + j));
        const char* src = (const char*)wo + (size_t)krow * 1280 + cb * 160 + ck * 16;
        if (lane < 50) { UNR for (int pd = 0; pd < 4; ++pd) glds16(src + pd * 160 * 1280, lds0 + L_WO + (pd * 32 + aw + 8 * i) * 800); }
      }
    }
    {
      const int aw = wid - 8, t = tid - 512;
      const int rgp = aw & 3, cs = aw >> 2;
      const int row = idx * 64 + rgp * 16 + l15;
      const int ck0 = cs * 4 + g, ck1 = 8 + (g & 1);
      const u32x4 q0 = *(const u32x4*)(qin + (size_t)row * 640 + xcd * 80 + ck0 * 8);
      const u32x4 q1 = *(const u32x4*)(qin + (size_t)row * 640 + xcd * 80 + ck1 * 8);
      __builtin_amdgcn_s_setprio(0);
      VM0();
      cnt_sig(lds0, C_W);
      if (wid == 8) {
        flags_wait(wr, WS_F3 + (xcd * 32 + (lane & 31)) * FSTRIDE, true, tag);
        cnt_add(lds0, C_YOK);
      }
      cnt_wait<4>(lds0, C_YOK, 1);
      __builtin_amdgcn_s_setprio(1);
      STAMP(9);
      const u32x4 y0 = ld128_sc1(wr, WS_Y2 + (xcd * 80 + (t >> 3)) * 4096 + idx * 128 + (t & 7) * 16);
      const int t2 = 512 + (t & 127);
      const u32x4 y1 = ld128_sc1(wr, WS_Y2 + (xcd * 80 + (t2 >> 3)) * 4096 + idx * 128 + (t2 & 7) * 16);
      lds_w128(lds0 + L_GT + (t >> 3) * 144 + (t & 7) * 16, y0);
      if (t < 128) lds_w128(lds0 + L_GT + (t2 >> 3) * 144 + (t2 & 7) * 16, y1);
      cnt_add(lds0, C_G);
      cnt_wait(lds0, C_G, 8);
      STAMP(10);
      const u32 ga = lds0 + L_GT + (l15 >> 2) * 144 + (rgp * 16 + 4 * (lane & 3)) * 2;
      u32x2 a0 = lds_tr(ga + ck0 * 8 * 144), a1 = lds_tr(ga + ck0 * 8 * 144 + 4 * 144);
      u32x2 b0 = lds_tr(ga + ck1 * 8 * 144), b1 = lds_tr(ga + ck1 * 8 * 144 + 4 * 144);
      asm volatile("" : "+v"(a0), "+v"(a1), "+v"(b0), "+v"(b1));
      out128(orr, (row * 1280 + xcd * 80 + ck0 * 8) * 2,
          u32x4{pack2(bf_lo(q0[0]) * bf_lo(a0[0]), bf_hi(q0[0]) * bf_hi(a0[0])), pack2(bf_lo(q0[1]) * bf_lo(a0[1]), bf_hi(q0[1]) * bf_hi(a0[1])),
                pack2(bf_lo(q0[2]) * bf_lo(a1[0]), bf_hi(q0[2]) * bf_hi(a1[0])), pack2(bf_lo(q0[3]) * bf_lo(a1[1]), bf_hi(q0[3]) * bf_hi(a1[1]))});
      if (cs == 0 && g < 2)
        out128(orr, (row * 1280 + xcd * 80 + ck1 * 8) * 2,
            u32x4{pack2(bf_lo(q1[0]) * bf_lo(b0[0]), bf_hi(q1[0]) * bf_hi(b0[0])), pack2(bf_lo(q1[1]) * bf_lo(b0[1]), bf_hi(q1[1]) * bf_hi(b0[1])),
                  pack2(bf_lo(q1[2]) * bf_lo(b1[0]), bf_hi(q1[2]) * bf_hi(b1[0])), pack2(bf_lo(q1[3]) * bf_lo(b1[1]), bf_hi(q1[3]) * bf_hi(b1[1]))});
    }
    __builtin_amdgcn_s_setprio(0);
    STAMP(11);
    if (b == 0 && wid == 15) {
      UNR for (int j = 0; j < 4; ++j) flags_wait(wr, WS_F1 + (j * 64 + lane) * FSTRIDE, true, tag);
      flag_publish(wr, WS_EPOCH, tag);
    }
  }
  STAMP(15);
  STAMP_FLUSH(ws);
  asm volatile(".Lpf_end:" :: "v"(code_touch));
}

extern "C" void kernel_launch(void* const* d_in, const int* in_sizes, int n_in, void* d_out, int out_size, void* d_ws, size_t ws_size,
                              hipStream_t stream) {
  fused_kernel<<<256, 1024, 0, stream>>>((const u16*)d_in[0], (const u16*)d_in[1], (const u16*)d_in[2], (const u16*)d_in[3], (const u16*)d_in[4],
                                         (const u16*)d_in[5], (const u16*)d_in[6], (const u16*)d_in[7], (const u16*)d_in[8], (char*)d_ws, (u16*)d_out);
}
```

```cpp
#include <hip/hip_runtime.h>
#include <hip/hip_bf16.h>
#include <stdint.h>

typedef unsigned short u16;
typedef unsigned int u32;
typedef unsigned long long u64;
typedef __bf16 bf16x8 __attribute__((ext_vector_type(8)));
typedef __bf16 bf16x2 __attribute__((ext_vector_type(2)));
typedef float f32x4 __attribute__((ext_vector_type(4)));
typedef float f32x2 __attribute__((ext_vector_type(2)));
typedef u32 u32x2 __attribute__((ext_vector_type(2)));
typedef u32 u32x4 __attribute__((ext_vector_type(4)));
typedef short s16x4 __attribute__((ext_vector_type(4)));
typedef __amdgpu_buffer_rsrc_t rsrc_t;
#define LDS3 __attribute__((address_space(3)))
#define DI __device__ __forceinline__
#define UNR _Pragma("unroll")
#define SBAR() __builtin_amdgcn_sched_barrier(0)

constexpr u32 PLANE = 2048u * 640u * 2u;
constexpr u32 WS_XR = 0;
constexpr u32 WS_Q = 1 * PLANE;
constexpr u32 WS_K = 2 * PLANE;
constexpr u32 WS_V = 3 * PLANE;
constexpr u32 WS_O = 4 * PLANE;
constexpr u32 WS_Y2 = 5 * PLANE;
constexpr u32 WS_EPOCH = 6 * PLANE;
#define FSTRIDE 128
constexpr u32 WS_F1 = WS_EPOCH + 2048, WS_F2 = WS_F1 + 320 * FSTRIDE, WS_F3 = WS_F2 + 256 * FSTRIDE, WS_F4 = WS_F3 + 256 * FSTRIDE;
constexpr u32 WS_END = WS_F4 + 256 * FSTRIDE;
constexpr float QSCALE = 0.22360679774997896f * 1.4426950408889634f;

constexpr u32 L_STAGE = 32768, L_KV = 65536, L_HC = 98304, HC_STRIDE = 4160, HC_CH = 16640, L_XR = 148224, XR_CH = 4608, L_CNT = 162048, LDS_BYTES = 162304;
constexpr u32 L_AK = 0, L_AV = 24576, L_ONES = 45056, L_RED1 = 49152, L_RED2 = 65536;
constexpr u32 L_TAG = L_CNT + 240;
constexpr u32 L_WO = 45312, L_GT = 148224, L_RED3 = 0;
enum { C_B = 0, C_QKV, C_KV, C_O, C_OOK, C_XR, C_P0, C_P1, C_Q4, C_Y, C_YOK, C_G, C_W, C_R0, C_R1, C_R2, C_R3, C_S1, C_S2, C_H, C_TAG,
       C_FULL = 24, C_EMPTY = 36, C_TAGV = 48, C_NUM = 52 };
#define GATE_N 0
#define CLR_POS 0
constexpr u32 SENT = 0xffffffffu;
constexpr u32 SPIN_CAP = 1u << 22;
#define POLL_SLEEP 4

#define STAMP_DECL
#define STAMP(i)
#define CSTAMP(i)
#define STAMP_FLUSH(wsp)

DI u32 nosent(u32 v) { return v == SENT ? 0x7fc07fc0u : v; }
DI float bf_lo(u32 v) { return __uint_as_float(v << 16); }
DI float bf_hi(u32 v) { return __uint_as_float(v & 0xffff0000u); }
DI u32 pack2(float lo, float hi) { f32x2 v = {lo, hi}; return __builtin_bit_cast(u32, __builtin_convertvector(v, bf16x2)); }
DI f32x4 mfma16(u32x4 a, u32x4 b, f32x4 c) {
  return __builtin_amdgcn_mfma_f32_16x16x32_bf16(__builtin_bit_cast(bf16x8, a), __builtin_bit_cast(bf16x8, b), c, 0, 0, 0);
}
#define ST_AUX 16
#define LD_AUX 16
#define LD_POL "sc1"
DI void glds16(const void* g, u32 lds_base) {
  u32 sv;
  asm volatile("s_mov_b32 %0, m0\n\ts_mov_b32 m0, %2\n\ts_nop 0\n\tglobal_load_lds_dwordx4 %1, off\n\ts_mov_b32 m0, %0"
               : "=&s"(sv) : "v"(g), "s"(lds_base) : "memory");
}
DI void glds16_sb(u32 voff, const void* sbase, u32 lds_base) {
  u32 sv;
  asm volatile("s_mov_b32 %0, m0\n\ts_mov_b32 m0, %3\n\ts_nop 0\n\tglobal_load_lds_dwordx4 %1, %2\n\ts_mov_b32 m0, %0"
               : "=&s"(sv) : "v"(voff), "s"(sbase), "s"(lds_base) : "memory");
}
DI void glds16_sc1(const void* g, u32 lds_base) {
  u32 sv;
  asm volatile("s_mov_b32 %0, m0\n\ts_mov_b32 m0, %2\n\ts_nop 0\n\tglobal_load_lds_dwordx4 %1, off " LD_POL "\n\ts_mov_b32 m0, %0"
               : "=&s"(sv) : "v"(g), "s"(lds_base) : "memory");
}
DI u32x4 lds_r128(u32 a) { return *(const LDS3 u32x4*)(uintptr_t)a; }
DI u32x2 lds_r64(u32 a) { return *(const LDS3 u32x2*)(uintptr_t)a; }
DI u32x2 lds_r64v(u32 a) { return *(const volatile LDS3 u32x2*)(uintptr_t)a; }
DI void lds_w128(u32 a, u32x4 v) { *(LDS3 u32x4*)(uintptr_t)a = v; }
DI u32x2 lds_tr(u32 a) { return __builtin_bit_cast(u32x2, __builtin_amdgcn_ds_read_tr16_b64_v4i16((LDS3 s16x4*)(uintptr_t)a)); }
DI void st128_sc1(rsrc_t r, u32 off, u32x4 v) { __builtin_amdgcn_raw_buffer_store_b128(v, r, off, 0, ST_AUX); }
DI void st64_sc1(rsrc_t r, u32 off, u32x2 v) { __builtin_amdgcn_raw_buffer_store_b64(v, r, off, 0, 16); }
DI u32x4 ld128_sc1(rsrc_t r, u32 off) { return __builtin_amdgcn_raw_buffer_load_b128(r, off, 0, LD_AUX); }
DI u32x2 ld64_sc1(rsrc_t r, u32 off) { return __builtin_amdgcn_raw_buffer_load_b64(r, off, 0, 16); }
#define VM_BAR(N) asm volatile("s_waitcnt vmcnt(" #N ") lgkmcnt(0)\n\ts_barrier" ::: "memory")
#define LG_BAR() asm volatile("s_waitcnt lgkmcnt(0)\n\ts_barrier" ::: "memory")
#define VM0() asm volatile("s_waitcnt vmcnt(0)" ::: "memory")

DI u32 cnt_add(u32 lds0, int c) {
  asm volatile("s_waitcnt lgkmcnt(0)" ::: "memory");
  u32 old = 0;
  if ((threadIdx.x & 63) == 0) old = __hip_atomic_fetch_add((LDS3 u32*)(uintptr_t)(lds0 + L_CNT + 4 * c), 1u, __ATOMIC_RELAXED, __HIP_MEMORY_SCOPE_WORKGROUP);
  return __builtin_amdgcn_readfirstlane(old);
}
DI void cnt_sig(u32 lds0, int c) {
  asm volatile("s_waitcnt lgkmcnt(0)" ::: "memory");
  if ((threadIdx.x & 63) == 0) (void)__hip_atomic_fetch_add((LDS3 u32*)(uintptr_t)(lds0 + L_CNT + 4 * c), 1u, __ATOMIC_RELAXED, __HIP_MEMORY_SCOPE_WORKGROUP);
  asm volatile("" ::: "memory");
}
template <int SLP = 1>
DI void cnt_wait(u32 lds0, int c, u32 n) {
  _Pragma("unroll 1") for (u32 spins = 0; spins < SPIN_CAP; ++spins) {
    if (__builtin_amdgcn_readfirstlane(__hip_atomic_load((LDS3 u32*)(uintptr_t)(lds0 + L_CNT + 4 * c), __ATOMIC_RELAXED, __HIP_MEMORY_SCOPE_WORKGROUP)) >= n) break;
    __builtin_amdgcn_s_sleep(SLP);
  }
  asm volatile("" ::: "memory");
}
DI void flag_publish(rsrc_t r, u32 off, u32x2 tag) { if ((threadIdx.x & 63) == 0) st64_sc1(r, off, tag); }
DI bool flags_ok(u32x2 v, u32x2 tag, bool need) { return __all(!need || (v[0] == tag[0] && v[1] == tag[1])); }
DI void flags_wait(rsrc_t r, u32 off, bool need, u32x2 tag) {
  _Pragma("unroll 1") for (u32 spins = 0; spins < SPIN_CAP; ++spins) {
    asm volatile("" ::: "memory");
    const u32x2 v = ld64_sc1(r, off);
    if (flags_ok(v, tag, need)) break;
    __builtin_amdgcn_s_sleep(POLL_SLEEP);
  }
  asm volatile("" ::: "memory");
}
#define OUT_AUX 2
DI void out128(rsrc_t r, u32 off, u32x4 v) { __builtin_amdgcn_raw_buffer_store_b128(v, r, off, 0, OUT_AUX); }
DI void out64(rsrc_t r, u32 off, u32x2 v) { __builtin_amdgcn_raw_buffer_store_b64(v, r, off, 0, OUT_AUX); }
DI u32x4 swap_pack(u32 a0, u32 a1, u32 b0, u32 b1) {
  auto s0 = __builtin_amdgcn_permlane16_swap(a0, b0, false, false);
  auto s1 = __builtin_amdgcn_permlane16_swap(a1, b1, false, false);
  return u32x4{s0[0], s1[0], s0[1], s1[1]};
}
DI float xmax4(float m) {
  auto r = __builtin_amdgcn_permlane32_swap(__float_as_uint(m), __float_as_uint(m), false, false);
  m = fmaxf(__uint_as_float(r[0]), __uint_as_float(r[1]));
  auto s = __builtin_amdgcn_permlane16_swap(__float_as_uint(m), __float_as_uint(m), false, false);
  return fmaxf(__uint_as_float(s[0]), __uint_as_float(s[1]));
}
DI float ex2(float v) { return __builtin_amdgcn_exp2f(v); }

DI void o_clear(rsrc_t wr, u32 obase, int head) {
  int lane = threadIdx.x & 63; asm volatile("" : "+v"(lane));
  const int l15 = lane & 15, g = lane >> 4;
  st128_sc1(wr, obase + (g & 1) * 20480 + (2 * head + (g >> 1)) * 256 + l15 * 16, u32x4{SENT, SENT, SENT, SENT});
  if (lane < 16) st128_sc1(wr, obase + (lane >> 3) * 20480 + 16384 + head * 128 + (lane & 7) * 16, u32x4{SENT, SENT, SENT, SENT});
}
template <bool SWAP>
DI void gemm1_compute(u32 lds0, rsrc_t wr, int wid, int lane, int rt, int ct, u32 obase, int hodd) {
  asm volatile("" : "+v"(lane));
  const int l15 = lane & 15, g = lane >> 4, wm = wid >> 2, wn = wid & 3;
  const u32 xa0 = lds0 + (wm * 64 + l15) * 128 + ((g ^ (lane & 7)) * 16), xa1 = xa0 ^ 64;
  u32 wa[2][2];
  {
    const int q = l15 >> 2, p = lane & 3;
    UNR for (int nt = 0; nt < 2; ++nt) UNR for (int t = 0; t < 2; ++t) {
      const int swz = (q << 2) | ((2 * g + t) & 3);
      wa[nt][t] = lds0 + 16384 + 256 * (8 * g + 4 * t + q) + 16 * ((2 * (wn * 2 + nt) + (p >> 1)) ^ swz) + 8 * (p & 1);
    }
  }
  f32x4 acc[4][2];
  UNR for (int i = 0; i < 4; ++i) UNR for (int j = 0; j < 2; ++j) acc[i][j] = f32x4{0.f, 0.f, 0.f, 0.f};
  u32x4 xf[2][4], wf[2][2];
#define G1_RW(set, kt, ks, nt) do { const u32 sb_ = ((kt) % 3) * L_STAGE; \
    const u32x2 lo = lds_tr(wa[nt][0] + sb_ + (ks) * 8192), hi = lds_tr(wa[nt][1] + sb_ + (ks) * 8192); wf[set][nt] = u32x4{lo[0], lo[1], hi[0], hi[1]}; } while (0)
#define G1_RX(set, kt, ks, mt) xf[set][mt] = lds_r128(((ks) ? xa1 : xa0) + ((kt) % 3) * L_STAGE + (mt) * 2048)
#define G1_M(set, mt, nt) acc[mt][nt] = SWAP ? mfma16(wf[set][nt], xf[set][mt], acc[mt][nt]) : mfma16(xf[set][mt], wf[set][nt], acc[mt][nt])
#define G1_HALF(cs, ns, kt, ks, RD) do { \
    G1_M(cs, 0, 0); if (RD) G1_RW(ns, kt, ks, 0); SBAR(); \
    G1_M(cs, 0, 1); if (RD) { G1_RX(ns, kt, ks, 0); G1_RX(ns, kt, ks, 1); } SBAR(); \
    G1_M(cs, 1, 0); if (RD) G1_RW(ns, kt, ks, 1); SBAR(); \
    G1_M(cs, 1, 1); if (RD) G1_RX(ns, kt, ks, 2); SBAR(); \
    G1_M(cs, 2, 0); if (RD) G1_RX(ns, kt, ks, 3); SBAR(); \
    G1_M(cs, 2, 1); G1_M(cs, 3, 0); G1_M(cs, 3, 1); SBAR(); } while (0)
  G1_RW(0, 0, 0, 0); G1_RX(0, 0, 0, 0); G1_RW(0, 0, 0, 1); G1_RX(0, 0, 0, 1); G1_RX(0, 0, 0, 2); G1_RX(0, 0, 0, 3);
  SBAR();
  UNR for (int t = 0; t < 10; ++t) {
    G1_HALF(0, 1, t, 1, true);
    if (t < 9) {
      LG_BAR();
      SBAR();
    }
    G1_HALF(1, 0, t + 1, 0, t < 9);
  }
  if (SWAP) {
    const float sc = ct < 5 ? QSCALE : 1.0f;
    const u32 base = ct < 5 ? WS_Q : WS_K;
    UNR for (int nt = 0; nt < 2; ++nt) {
      const int G8 = ((ct % 5) * 128 + wn * 32 + nt * 16) / 8 + (g >> 1);
      const int pr = G8 / 5, gr = G8 - pr * 5;
      UNR for (int mp = 0; mp < 2; ++mp) {
        const f32x4 a = acc[2 * mp][nt], b = acc[2 * mp + 1][nt];
        const u32x4 v = swap_pack(pack2(a[0] * sc, a[1] * sc), pack2(a[2] * sc, a[3] * sc), pack2(b[0] * sc, b[1] * sc), pack2(b[2] * sc, b[3] * sc));
        const int tile = rt * 8 + wm * 4 + 2 * mp + (g & 1);
        st128_sc1(wr, base + ((pr * 128 + tile) * 5 + gr) * 256 + l15 * 16, v);
      }
    }
  } else {
    UNR for (int nt = 0; nt < 2; ++nt) {
      const int c = (ct - 10) * 128 + wn * 32 + nt * 16 + l15;
      const int h = c / 20, d = c - h * 20;
      const int off = d < 16 ? g * 256 + d * 16 : 1024 + g * 64 + (d - 16) * 16;
      UNR for (int mp = 0; mp < 2; ++mp) {
        const int blk = rt * 4 + wm * 2 + mp;
        const f32x4 a = acc[2 * mp][nt], b = acc[2 * mp + 1][nt];
        st128_sc1(wr, WS_V + (h * 64 + blk) * 1280 + off, u32x4{pack2(a[0], a[1]), pack2(a[2], a[3]), pack2(b[0], b[1]), pack2(b[2], b[3])});
      }
    }
  }
}

struct AttnOut { f32x4 oA0, oA1, oB0, oB1; };

DI void attn_slow(u32 ka, u32 va0, u32 va1, u32 v1inc, u32x4 qA, u32x4 qB, int kb0, int l15, int g, AttnOut& o) {
  const f32x4 z = f32x4{0.f, 0.f, 0.f, 0.f};
  float tA = -INFINITY, tB = -INFINITY;
  for (int t = 2 * kb0; t < 18; ++t) {
    const u32x4 kf = lds_r128(ka + t * 768);
    const f32x4 sA = mfma16(kf, qA, z), sB = mfma16(kf, qB, z);
    UNR for (int r = 0; r < 4; ++r) {
      const int dA = 16 * t + 4 * g + r - l15;
      tA = fmaxf(tA, (dA >= 1 && dA <= 256) ? sA[r] : -INFINITY);
      tB = fmaxf(tB, (dA >= 17 && dA <= 272) ? sB[r] : -INFINITY);
    }
  }
  const float mA = xmax4(tA), mB = xmax4(tB);
  o.oA0 = o.oA1 = o.oB0 = o.oB1 = z;
  for (int kbk = kb0; kbk < 9; ++kbk) {
    float pa[8], pb[8];
    UNR for (int hf = 0; hf < 2; ++hf) {
      const int t = 2 * kbk + hf;
      const u32x4 kf = lds_r128(ka + t * 768);
      const f32x4 sA = mfma16(kf, qA, z), sB = mfma16(kf, qB, z);
      UNR for (int r = 0; r < 4; ++r) {
        const int dA = 16 * t + 4 * g + r - l15;
        pa[4 * hf + r] = (dA >= 1 && dA <= 256) ? ex2(sA[r] - mA) : 0.f;
        pb[4 * hf + r] = (dA >= 17 && dA <= 272) ? ex2(sB[r] - mB) : 0.f;
      }
    }
    const u32x4 vf0 = lds_r128(va0 + kbk * 1280), vf1 = lds_r128(va1 + kbk * v1inc);
    const u32x4 pA = u32x4{pack2(pa[0], pa[1]), pack2(pa[2], pa[3]), pack2(pa[4], pa[5]), pack2(pa[6], pa[7])};
    const u32x4 pB = u32x4{pack2(pb[0], pb[1]), pack2(pb[2], pb[3]), pack2(pb[4], pb[5]), pack2(pb[6], pb[7])};
    o.oA0 = mfma16(vf0, pA, o.oA0); o.oA1 = mfma16(vf1, pA, o.oA1);
    o.oB0 = mfma16(vf0, pB, o.oB0); o.oB1 = mfma16(vf1, pB, o.oB1);
  }
}

DI void attn_fast(u32 ka, u32 va0, u32 va1, u32 v1inc, u32x4 qA, u32x4 qB, int l15, int g, AttnOut& o) {
  const f32x4 z = f32x4{0.f, 0.f, 0.f, 0.f};
  bool lo[4];
  UNR for (int r = 0; r < 4; ++r) lo[r] = 4 * g + r <= l15;
  u32x4 kf[2][2], vf[2][2], pA, pB;
  f32x4 S[2][4];
  kf[1][0] = lds_r128(ka + 16 * 768); kf[1][1] = lds_r128(ka + 17 * 768);
  kf[0][0] = lds_r128(ka); kf[0][1] = lds_r128(ka + 768);
  vf[1][0] = lds_r128(va0 + 8 * 1280); vf[1][1] = lds_r128(va1 + 8 * v1inc);
  f32x4 cA, cB;
  {
    const f32x4 sA = mfma16(kf[1][0], qA, z), sB = mfma16(kf[1][1], qB, z), sB0 = mfma16(kf[1][0], qB, z);
    SBAR();
    kf[1][0] = lds_r128(ka + 2 * 768); kf[1][1] = lds_r128(ka + 3 * 768);
    float mA = -INFINITY, mB = -INFINITY;
    UNR for (int r = 0; r < 4; ++r) { mA = fmaxf(mA, lo[r] ? sA[r] : -INFINITY); mB = fmaxf(mB, lo[r] ? sB[r] : -INFINITY); }
    mA = xmax4(mA); mB = xmax4(mB);
    cA = f32x4{-mA, -mA, -mA, -mA}; cB = f32x4{-mB, -mB, -mB, -mB};
    S[0][0] = mfma16(kf[0][0], qA, cA); S[0][1] = mfma16(kf[0][1], qA, cA); S[0][3] = mfma16(kf[0][1], qB, cB);
    float a[4], b0[4], b1[4];
    UNR for (int r = 0; r < 4; ++r) { a[r] = lo[r] ? ex2(sA[r] - mA) : 0.f; b0[r] = ex2(sB0[r] - mB); b1[r] = lo[r] ? ex2(sB[r] - mB) : 0.f; }
    pA = u32x4{pack2(a[0], a[1]), pack2(a[2], a[3]), 0u, 0u};
    pB = u32x4{pack2(b0[0], b0[1]), pack2(b0[2], b0[3]), pack2(b1[0], b1[1]), pack2(b1[2], b1[3])};
  }
  o.oA0 = o.oA1 = o.oB0 = o.oB1 = z;
  SBAR();
  UNR for (int k = 0; k < 8; ++k) {
    const int c = k & 1, n = c ^ 1;
    vf[c][0] = lds_r128(va0 + k * 1280); vf[c][1] = lds_r128(va1 + k * v1inc);
    if (k < 6) { kf[c][0] = lds_r128(ka + (2 * k + 4) * 768); kf[c][1] = lds_r128(ka + (2 * k + 5) * 768); }
    SBAR();
    u32 np[8];
    UNR for (int i = 0; i < 4; ++i) {
      if (k < 7) S[n][i] = mfma16(kf[n][i & 1], i < 2 ? qA : qB, i < 2 ? cA : cB);
      float e[4];
      if (k == 0 && i == 2) { np[4] = 0u; np[5] = 0u; }
      else {
        e[0] = ex2(S[c][i][0]); e[1] = ex2(S[c][i][1]);
      }
      if (i == 0) o.oA0 = mfma16(vf[n][0], pA, o.oA0);
      if (i == 1) o.oA1 = mfma16(vf[n][1], pA, o.oA1);
      if (i == 2) o.oB0 = mfma16(vf[n][0], pB, o.oB0);
      if (i == 3) o.oB1 = mfma16(vf[n][1], pB, o.oB1);
      if (!(k == 0 && i == 2)) {
        e[2] = ex2(S[c][i][2]); e[3] = ex2(S[c][i][3]);
        if (k == 0 && (i == 0 || i == 3)) { UNR for (int r = 0; r < 4; ++r) e[r] = lo[r] ? 0.f : e[r]; }
        np[2 * i] = pack2(e[0], e[1]); np[2 * i + 1] = pack2(e[2], e[3]);
      }
      SBAR();
    }
    pA = u32x4{np[0], np[1], np[2], np[3]}; pB = u32x4{np[4], np[5], np[6], np[7]};
  }
  o.oA0 = mfma16(vf[1][0], pA, o.oA0); o.oA1 = mfma16(vf[1][1], pA, o.oA1);
  o.oB0 = mfma16(vf[1][0], pB, o.oB0); o.oB1 = mfma16(vf[1][1], pB, o.oB1);
}

template <int TMASK, int NR>
DI void conv_accum(u32 ha, u32 xa, f32x4 (&acc)[8]) {
  constexpr int NF = (TMASK & 0xC0) ? 8 : 6;
  u32x4 A[8], B[8];
#define CV_LA(i, rr) do { if ((i) < NF) { const u32x2 lo_ = lds_r64v(ha + (rr) * 64 + (i) * 512), hi_ = lds_r64v(ha + (rr) * 64 + (i) * 512 + 8); \
    A[i] = u32x4{lo_[0], lo_[1], hi_[0], hi_[1]}; } } while (0)
#define CV_LB(m, rr) do { if ((m) < NF) B[m] = lds_r128(xa + (rr) * 64 + (7 - (m)) * 512); } while (0)
#define CV_MM(i, m) do { if ((i) < NF && (m) < NF && (i) + (m) < 8 && ((TMASK >> ((i) + (m))) & 1)) acc[(i) + (m)] = mfma16(A[i], B[m], acc[(i) + (m)]); } while (0)
  CV_LB(0, 0); UNR for (int i = 7; i >= 4; --i) CV_LA(i, 0);
  UNR for (int m = 1; m < 4; ++m) CV_LB(m, 0);
  UNR for (int i = 0; i < 4; ++i) { CV_LA(i, 0); CV_LB(7 - i, 0); }
  SBAR();
  UNR for (int rr = 0; rr < NR; ++rr) {
    UNR for (int m = 0; m < 4; ++m) UNR for (int i = 7; i >= 4; --i) CV_MM(i, m);
    SBAR();
    if (rr + 1 < NR) { UNR for (int i = 7; i >= 4; --i) CV_LA(i, rr + 1); }
    SBAR();
    UNR for (int i = 0; i < 4; ++i) UNR for (int m = 7; m >= 4; --m) CV_MM(i, m);
    SBAR();
    if (rr + 1 < NR) { UNR for (int m = 7; m >= 4; --m) CV_LB(m, rr + 1); }
    SBAR();
    UNR for (int m = 0; m < 4; ++m) {
      UNR for (int i = 0; i < 4; ++i) CV_MM(i, m);
      SBAR();
      if (rr + 1 < NR) CV_LB(m, rr + 1);
      SBAR();
    }
    if (rr + 1 < NR) { UNR for (int i = 0; i < 4; ++i) CV_LA(i, rr + 1); }
    SBAR();
  }
}
DI u32x2 conv_fin(f32x4 a, int n, u32 xbase, float dcoef, int l15, int g) {
  const int s = 256 * n + 16 * l15 + 4 * g;
  const u32x2 xv = lds_r64(xbase + (2044 - s) * 2);
  const u32 y01 = pack2(a[0], a[1]), y23 = pack2(a[2], a[3]);
  const u32 d01 = pack2(bf_hi(xv[1]) * dcoef, bf_lo(xv[1]) * dcoef), d23 = pack2(bf_hi(xv[0]) * dcoef, bf_lo(xv[0]) * dcoef);
  return u32x2{pack2(bf_lo(y01) + bf_lo(d01), bf_hi(y01) + bf_hi(d01)), pack2(bf_lo(y23) + bf_lo(d23), bf_hi(y23) + bf_hi(d23))};
}
DI void conv_store2(rsrc_t wr, u32 rowoff, u32x2 ya, int na, u32x2 yb, int nb, int l15, int g) {
  const u32x4 v = swap_pack(ya[0], ya[1], yb[0], yb[1]);
  st128_sc1(wr, rowoff + (256 * ((g & 1) ? nb : na) + 16 * l15 + 8 * (g >> 1)) * 2, v);
}
DI void conv_store1(rsrc_t wr, u32 rowoff, u32x2 ya, int na, int l15, int g) { st64_sc1(wr, rowoff + (256 * na + 16 * l15 + 4 * g) * 2, ya); }

DI u32 kv_pair(u32 k0, u32 k1, u32 k2, u32 k3, u32 v, u32 sa01, u32 sa23, u32 sb01, u32 sb23) {
  u32 p = pack2(bf_lo(k0) * bf_hi(sa23), bf_hi(k0) * bf_hi(sb23));
  u32 q = pack2(bf_lo(k1) * bf_lo(sa23), bf_hi(k1) * bf_lo(sb23));
  p = pack2(bf_lo(p) + bf_lo(q), bf_hi(p) + bf_hi(q));
  q = pack2(bf_lo(k2) * bf_hi(sa01), bf_hi(k2) * bf_hi(sb01));
  p = pack2(bf_lo(p) + bf_lo(q), bf_hi(p) + bf_hi(q));
  q = pack2(bf_lo(k3) * bf_lo(sa01), bf_hi(k3) * bf_lo(sb01));
  p = pack2(bf_lo(p) + bf_lo(q), bf_hi(p) + bf_hi(q));
  return pack2(bf_lo(p) * bf_lo(v), bf_hi(p) * bf_hi(v));
}
DI u32 lo2(u32 a, u32 b) { return __builtin_amdgcn_perm(b, a, 0x05040100u); }
DI u32 hi2(u32 a, u32 b) { return __builtin_amdgcn_perm(b, a, 0x07060302u); }

__global__ __launch_bounds__(1024, 4) void fused_kernel(const u16* __restrict__ qin, const u16* __restrict__ kin, const u16* __restrict__ vin,
                                                         const u16* __restrict__ shk, const u16* __restrict__ ssmk, const u16* __restrict__ ssmD,
                                                         const u16* __restrict__ x, const u16* __restrict__ wqkv, const u16* __restrict__ wo,
                                                         char* __restrict__ ws, u16* __restrict__ out) {
  __shared__ __attribute__((aligned(1024))) char lds[LDS_BYTES];
  const u32 lds0 = (u32)(uintptr_t)lds;
  int tid = threadIdx.x, lane = tid & 63;
  const int wid = __builtin_amdgcn_readfirstlane(tid >> 6);
  int l15 = lane & 15, g = lane >> 4;
#define FRESH_IDS() do { asm volatile("" : "+v"(tid)); lane = tid & 63; l15 = lane & 15; g = lane >> 4; } while (0)
  const int b = blockIdx.x, xcd = b & 7, idx = b >> 3;
  const rsrc_t wr = __builtin_amdgcn_make_buffer_rsrc(ws, 0, WS_END, 0x00020000);
  const rsrc_t orr = __builtin_amdgcn_make_buffer_rsrc(out, 0, 2048 * 1280 * 2, 0x00020000);
  STAMP_DECL; STAMP(0);
  u32 code_touch = 0;
  if (wid == 7) {
    u64 pc; u32 len;
    asm volatile("s_getpc_b64 %0\n.Lpf_here:\n\ts_mov_b32 %1, .Lpf_end-.Lpf_here" : "=s"(pc), "=s"(len));
    const u32 off = (u32)(idx * 16 + (lane & 15)) * 128u;
    u64 a = pc + (off + 4 <= len ? off : 0u);
    if (lane >= 16) {
      const u32 pg = (u32)(idx * 16 + (lane & 15));
      if (pg < 240) a = (u64)ws + (u64)pg * 65536u;
      else if (pg < 320) a = (u64)out + (u64)(pg - 240) * 65536u;
      else if (pg < 360) a = (u64)qin + (u64)(pg - 320) * 65536u;
      else if (pg < 373) a = (u64)wo + (u64)(pg - 360) * 65536u;
    }
    if (lane < 32) code_touch = __hip_atomic_load((const __attribute__((address_space(1))) u32*)a, __ATOMIC_RELAXED, __HIP_MEMORY_SCOPE_AGENT);
  }
  u32x2 tag;
  const int jr = xcd >> 1, jc = xcd & 1;
  const bool has_gemm = !(jc == 1 && idx >= 28);
  const int rt = jr * 4 + (idx & 3), ct = jc * 8 + (idx >> 2);
  const int cbase = 80 * xcd + 5 * (idx >> 1), odd = idx & 1;
  const int ch0 = cbase + (odd ? 3 : 0), ch2 = cbase + 2;

  const int head = xcd * 4 + (idx >> 3), qb = idx & 7, pr = head >> 1, g0 = 2 * (head & 1);
  if (wid < 8) {
    {
      const int it = tid < 320 ? tid : tid - 192;
      const int cq = it % 20, rg = it / 20;
      const int s0 = idx * 64 + rg * 4, c0 = xcd * 80 + cq * 4;
      u32x2 kk[7], vv[4];
      u32x4 sh0, sh1;
      if (wid < 5) {
        UNR for (int j = 0; j < 7; ++j) { const int r = s0 - 3 + j; kk[j] = __builtin_nontemporal_load((const u32x2*)(kin + (size_t)(r < 0 ? 0 : r) * 640 + c0)); }
        UNR for (int j = 0; j < 4; ++j) vv[j] = __builtin_nontemporal_load((const u32x2*)(vin + (size_t)(s0 + j) * 640 + c0));
        sh0 = *(const u32x4*)(shk + c0 * 4); sh1 = *(const u32x4*)(shk + c0 * 4 + 8);
      }
      const u32x2 ep = ld64_sc1(wr, WS_EPOCH);
      if (tid >= 320 && tid < 320 + C_NUM) *(LDS3 u32*)(uintptr_t)(lds0 + L_CNT + (tid - 320) * 4) = 0u;
      if (tid >= 384 && tid < 480) lds_w128(lds0 + L_XR + ((tid - 384) >> 5) * XR_CH + 4096 + (tid & 31) * 16, u32x4{0u, 0u, 0u, 0u});
      LG_BAR();
      if (wid < 5) {
        if (s0 == 0) { kk[0] = u32x2{0u, 0u}; kk[1] = u32x2{0u, 0u}; kk[2] = u32x2{0u, 0u}; }
        u32 r01[4], r23[4];
        UNR for (int j = 0; j < 4; ++j) {
          r01[j] = kv_pair(kk[j][0], kk[j + 1][0], kk[j + 2][0], kk[j + 3][0], vv[j][0], sh0[0], sh0[1], sh0[2], sh0[3]);
          r23[j] = kv_pair(kk[j][1], kk[j + 1][1], kk[j + 2][1], kk[j + 3][1], vv[j][1], sh1[0], sh1[1], sh1[2], sh1[3]);
        }
        const u32 a = lds0 + L_KV + (cq * 4) * 144 + (60 - 4 * rg) * 2;
        *(LDS3 u32x2*)(uintptr_t)(a) = u32x2{lo2(r01[3], r01[2]), lo2(r01[1], r01[0])};
        *(LDS3 u32x2*)(uintptr_t)(a + 144) = u32x2{hi2(r01[3], r01[2]), hi2(r01[1], r01[0])};
        *(LDS3 u32x2*)(uintptr_t)(a + 288) = u32x2{lo2(r23[3], r23[2]), lo2(r23[1], r23[0])};
        *(LDS3 u32x2*)(uintptr_t)(a + 432) = u32x2{hi2(r23[3], r23[2]), hi2(r23[1], r23[0])};
      }
      const u64 e = (((u64)ep[1] << 32) | ep[0]) + 1;
      tag = u32x2{(u32)__builtin_amdgcn_readfirstlane((u32)e), (u32)__builtin_amdgcn_readfirstlane((u32)(e >> 32))};
      if (tid == 448) *(LDS3 u32x2*)(uintptr_t)(lds0 + L_TAG) = tag;
      asm volatile("" :: "v"(code_touch));
      STAMP(11);
      cnt_sig(lds0, C_S1); cnt_wait(lds0, C_S1, 8);
      STAMP(12);
      st128_sc1(wr, WS_XR + (xcd * 80 + (tid >> 3)) * 4096 + (1984 - 64 * idx) * 2 + (tid & 7) * 16, lds_r128(lds0 + L_KV + (tid >> 3) * 144 + (tid & 7) * 16));
      if (tid < 128) st128_sc1(wr, WS_XR + (xcd * 80 + 64 + (tid >> 3)) * 4096 + (1984 - 64 * idx) * 2 + (tid & 7) * 16,
                               lds_r128(lds0 + L_KV + (64 + (tid >> 3)) * 144 + (tid & 7) * 16));
      VM0();
      if (cnt_add(lds0, C_H) == 7) flag_publish(wr, WS_F1 + (xcd * 32 + idx) * FSTRIDE, tag);
      LG_BAR();
      STAMP(13);
      FRESH_IDS();
      const u32 obase = WS_O + (qb * 16 + wid * 2) * 20480 + 5 * pr * 256;
      if (has_gemm) {
        if (ct < 10) gemm1_compute<true>(lds0, wr, wid, lane, rt, ct, obase, head & 1);
        else gemm1_compute<false>(lds0, wr, wid, lane, rt, ct, obase, head & 1);
      } else {
        UNR for (int t = 1; t < 10; ++t) LG_BAR();
      }
    }
    STAMP(1);
    VM0();
    if (cnt_add(lds0, C_B) == 7) {
      if (has_gemm) flag_publish(wr, WS_F2 + (rt * 15 + ct) * FSTRIDE, tag);
    }
    STAMP(2);
    FRESH_IDS();
    if (wid == 0) {
      const int which = lane >> 3, r = (lane >> 1) & 3, hi = lane & 1;
      const int prt = 2 * qb - 2 + r, pct = 5 * which + ((20 * head + (hi ? 19 : 0)) >> 7);
      const bool need = lane < 24 && prt >= 0 && !(which == 0 && r < 2);
      flags_wait(wr, WS_F2 + (need ? (prt * 15 + pct) * FSTRIDE : 0), need, tag);
      cnt_add(lds0, C_QKV);
    }
    cnt_wait(lds0, C_B, 8);
    if (qb == 0) {
      lds_w128(lds0 + L_AK + tid * 16, u32x4{0u, 0u, 0u, 0u});
      if (tid < 256) lds_w128(lds0 + L_AK + 8192 + tid * 16, u32x4{0u, 0u, 0u, 0u});
      lds_w128(lds0 + L_AV + tid * 16, u32x4{0u, 0u, 0u, 0u});
      if (tid < 128) lds_w128(lds0 + L_AV + 8192 + tid * 16, u32x4{0u, 0u, 0u, 0u});
    }
    if (tid < 16) { const u32 o1 = (qb > 0 || tid >= 8) ? 0x3f803f80u : 0u; lds_w128(lds0 + L_ONES + tid * 16, u32x4{o1, o1, o1, o1}); }
    const char* ksrc[3];
    UNR for (int j = 0; j < 3; ++j) {
      const int s = 64 * j + lane, i = s / 48, u = s - i * 48;
      ksrc[j] = ws + WS_K + (((pr * 128 + qb * 16 - 16 + 4 * wid + i) * 5 + g0) * 256 + u * 16);
      asm volatile("" : "+v"(ksrc[j]));
    }
    const char* vsrc = ws + WS_V + ((head * 64 + qb * 8 - 8) * 1280 + wid * 1024 + lane * 16);
    asm volatile("" : "+v"(vsrc));
    cnt_wait<2>(lds0, C_QKV, 1);
    STAMP(3);
    if (qb > 0 || wid >= 4) { UNR for (int j = 0; j < 3; ++j) glds16_sc1(ksrc[j], lds0 + L_AK + (3 * wid + j) * 1024); }
    UNR for (int j = 0; j < 3; ++j) {
      const int n = wid + 8 * j;
      if (n < 20 && (qb > 0 || n >= 10)) glds16_sc1(vsrc + j * 8192, lds0 + L_AV + n * 1024);
    }
    const int qt = qb * 16 + wid * 2, gq = g < 3 ? g : 2;
    u32x4 qA = ld128_sc1(wr, WS_Q + ((pr * 128 + qt) * 5 + g0 + gq) * 256 + l15 * 16);
    u32x4 qB = ld128_sc1(wr, WS_Q + ((pr * 128 + qt + 1) * 5 + g0 + gq) * 256 + l15 * 16);
    {
      const bool zlo = g == 3 || ((head & 1) && g == 0), zhi = g == 3 || (!(head & 1) && g == 2);
      if (zlo) { qA[0] = 0u; qA[1] = 0u; qB[0] = 0u; qB[1] = 0u; }
      if (zhi) { qA[2] = 0u; qA[3] = 0u; qB[2] = 0u; qB[3] = 0u; }
    }
    VM0();
    cnt_sig(lds0, C_KV);
    cnt_wait(lds0, C_KV, 8);
    STAMP(4);
    u32 tw;
    {
      const char* p1 = (const char*)wo + (size_t)(idx * 200 + (tid < 200 ? tid : 0)) * 128;
      const char* p2 = (const char*)qin + (size_t)(idx * 64 + ((tid & 127) >> 1)) * 1280 + xcd * 160 + (tid & 1) * 128;
      tw = *(const u32*)(tid < 256 ? p1 : p2);
    }
    {
      const u32 ka = lds0 + L_AK + wid * 1536 + gq * 256 + l15 * 16;
      const u32 va0 = lds0 + L_AV + wid * 1280 + g * 256 + l15 * 16;
      const u32 va1 = l15 < 4 ? lds0 + L_AV + wid * 1280 + 1024 + g * 64 + l15 * 16 : lds0 + L_ONES + wid * 16;
      const u32 v1inc = l15 < 4 ? 1280u : 16u;
      AttnOut o;
      attn_fast(ka, va0, va1, v1inc, qA, qB, l15, g, o);
      float lA = __shfl(o.oA1[0], 16 + l15), lB = __shfl(o.oB1[0], 16 + l15);
      if (__any(!(lA <= 16777216.f) || !(lB <= 16777216.f))) {
        attn_slow(ka, va0, va1, v1inc, qA, qB, qb == 0 ? 8 - wid : 0, l15, g, o);
        lA = __shfl(o.oA1[0], 16 + l15); lB = __shfl(o.oB1[0], 16 + l15);
      }
      asm volatile("" :: "v"(tw));
      const float iA = 1.0f / lA, iB = 1.0f / lB;
      cnt_wait(lds0, C_Y, 8);
      const u32x4 v = swap_pack(pack2(o.oA0[0] * iA, o.oA0[1] * iA), pack2(o.oA0[2] * iA, o.oA0[3] * iA),
                                pack2(o.oB0[0] * iB, o.oB0[1] * iB), pack2(o.oB0[2] * iB, o.oB0[3] * iB));
      st128_sc1(wr, WS_O + (qt + (g & 1)) * 20480 + (2 * head + (g >> 1)) * 256 + l15 * 16, u32x4{nosent(v[0]), nosent(v[1]), nosent(v[2]), nosent(v[3])});
      const u32x4 w = swap_pack(pack2(o.oA1[0] * iA, o.oA1[1] * iA), pack2(o.oA1[2] * iA, o.oA1[3] * iA),
                                pack2(o.oB1[0] * iB, o.oB1[1] * iB), pack2(o.oB1[2] * iB, o.oB1[3] * iB));
      if (g < 2) st64_sc1(wr, WS_O + (qt + g) * 20480 + 16384 + head * 128 + l15 * 8, u32x2{nosent(w[0]), nosent(w[1])});
    }
    STAMP(5);
    cnt_sig(lds0, C_O);
    STAMP(6);
    FRESH_IDS();
    const int rb = xcd * 4 + (idx & 3), cb = idx >> 2;
    if (wid == 0) {
      cnt_wait<2>(lds0, C_TAG, 1);
      const int hh = lane & 31;
      const u32 ho = WS_O + (rb * 4 + 2 * (lane >> 5) + 1) * 20480 + 16384 + hh * 128 + 120;
      _Pragma("unroll 1") for (u32 spins = 0; spins < SPIN_CAP; ++spins) {
        asm volatile("" ::: "memory");
        const u32x2 v = ld64_sc1(wr, ho);
        if (__all(v[0] != SENT && v[1] != SENT)) break;
        __builtin_amdgcn_s_sleep(POLL_SLEEP);
      }
      cnt_sig(lds0, C_OOK);
    }
    {
      const int mt = wid & 3, kh = wid >> 2;
      const u32 wa = lds0 + L_WO + kh * 51200 + (16 * (g >> 1) + 4 * (g & 1) + (l15 >> 2)) * 160 + (lane & 3) * 8;
      const u32 ob = WS_O + (rb * 4 + mt) * 20480;
      const u32 o2 = ob + (kh ? 16384 + g * 256 + l15 * 8 : 6144 + lane * 16), o2d = kh ? 128u : 8u;
      f32x4 acc[5];
      u32x4 wf[2][5];
#define G2_RW(set, ks) do { UNR for (int j = 0; j < 5; ++j) { const u32x2 lo = lds_tr(wa + (ks) * 5120 + j * 32), hi = lds_tr(wa + (ks) * 5120 + 1280 + j * 32); \
        wf[set][j] = u32x4{lo[0], lo[1], hi[0], hi[1]}; } } while (0)
      cnt_wait<2>(lds0, C_W, 8);
      if (wid >= 4) __builtin_amdgcn_s_setprio(1);
      _Pragma("unroll 1") for (u32 spins = 0; spins < SPIN_CAP; ++spins) {
        G2_RW(0, 0);
        SBAR();
        cnt_wait<1>(lds0, C_OOK, 1);
        STAMP(7);
        u32x4 of[10];
        UNR for (int ks = 0; ks < 6; ++ks) of[ks] = ld128_sc1(wr, ob + (kh * 10 + ks) * 1024 + lane * 16);
        UNR for (int ks = 6; ks < 10; ++ks) {
          const u32x2 lo = ld64_sc1(wr, o2 + (ks - 6) * 1024), hi = ld64_sc1(wr, o2 + (ks - 6) * 1024 + o2d);
          of[ks] = u32x4{lo[0], lo[1], hi[0], hi[1]};
        }
        SBAR();
        STAMP(8);
        UNR for (int j = 0; j < 5; ++j) acc[j] = f32x4{0.f, 0.f, 0.f, 0.f};
        u32 mx = 0;
        UNR for (int ks = 0; ks < 10; ++ks) {
          UNR for (int j = 0; j < 5; ++j) {
            if (ks < 9) { const u32x2 lo = lds_tr(wa + (ks + 1) * 5120 + j * 32), hi = lds_tr(wa + (ks + 1) * 5120 + 1280 + j * 32);
                          wf[(ks + 1) & 1][j] = u32x4{lo[0], lo[1], hi[0], hi[1]}; }
            acc[j] = mfma16(wf[ks & 1][j], of[ks], acc[j]);
            SBAR();
          }
          mx = max(max(mx, of[ks][0]), of[ks][1]); mx = max(max(mx, of[ks][2]), of[ks][3]);
        }
        if (!__any(mx == SENT)) break;
      }
      cnt_wait(lds0, C_O, 8);
      STAMP(9);
      const u32 red = lds0 + L_RED3 + mt * 5120 + lane * 16;
      if (kh == 0) { lds_w128(red + 2 * 1024, __builtin_bit_cast(u32x4, acc[2])); lds_w128(red + 3 * 1024, __builtin_bit_cast(u32x4, acc[3])); }
      else { lds_w128(red, __builtin_bit_cast(u32x4, acc[0])); lds_w128(red + 1024, __builtin_bit_cast(u32x4, acc[1])); lds_w128(red + 4 * 1024, __builtin_bit_cast(u32x4, acc[4])); }
      cnt_add(lds0, C_R0 + mt);
      cnt_wait(lds0, C_R0 + mt, 2);
      const u32 orow = ((rb * 64 + mt * 16 + l15) * 1280 + 640 + cb * 80) * 2;
      if (kh == 0) {
        const f32x4 a = acc[0] + __builtin_bit_cast(f32x4, lds_r128(red)), c = acc[1] + __builtin_bit_cast(f32x4, lds_r128(red + 1024));
        const f32x4 e = acc[4] + __builtin_bit_cast(f32x4, lds_r128(red + 4 * 1024));
        out128(orr, orow + ((g & 1) * 16 + (g >> 1) * 8) * 2, swap_pack(pack2(a[0], a[1]), pack2(a[2], a[3]), pack2(c[0], c[1]), pack2(c[2], c[3])));
        out64(orr, orow + (64 + 4 * g) * 2, u32x2{pack2(e[0], e[1]), pack2(e[2], e[3])});
      } else {
        const f32x4 a = acc[2] + __builtin_bit_cast(f32x4, lds_r128(red + 2 * 1024)), c = acc[3] + __builtin_bit_cast(f32x4, lds_r128(red + 3 * 1024));
        out128(orr, orow + (32 + (g & 1) * 16 + (g >> 1) * 8) * 2, swap_pack(pack2(a[0], a[1]), pack2(a[2], a[3]), pack2(c[0], c[1]), pack2(c[2], c[3])));
      }
    }
    STAMP(10);
  } else {
    {
      const int aw = wid - 8, t = tid - 512, tc = t & 255;
      u32x4 hd[2]; u32x2 he[2];
      UNR for (int c = 0; c < 2; ++c) {
        const u16* hp = ssmk + (size_t)(c == 0 ? ch0 + (t >> 8) : ch2) * 2048 + tc * 8;
        hd[c] = __builtin_nontemporal_load((const u32x4*)hp);
        he[c] = __builtin_nontemporal_load((const u32x2*)(hp + (tc < 255 ? 8 : 0)));
      }
#define H_WRITE(i, c) do { const u32x4 d = hd[i]; const u32x2 e = he[i]; \
        const u32 a01 = __builtin_amdgcn_alignbit(d[1], d[0], 16), a12 = __builtin_amdgcn_alignbit(d[2], d[1], 16), a23 = __builtin_amdgcn_alignbit(d[3], d[2], 16), \
                  a34 = __builtin_amdgcn_alignbit(e[0], d[3], 16), a45 = __builtin_amdgcn_alignbit(e[1], e[0], 16); \
        const u32 a = lds0 + L_HC + (c) * HC_CH + 32 + tc * 16; \
        lds_w128(a, d); lds_w128(a + HC_STRIDE, u32x4{a01, a12, a23, a34}); \
        lds_w128(a + 2 * HC_STRIDE, u32x4{d[1], d[2], d[3], e[0]}); lds_w128(a + 3 * HC_STRIDE, u32x4{a12, a23, a34, a45}); \
        if (tc == 0) { const u32 z = 0u; \
          lds_w128(a - 32, u32x4{z, z, z, z}); lds_w128(a - 16, u32x4{z, z, z, z}); \
          lds_w128(a - 32 + HC_STRIDE, u32x4{z, z, z, z}); lds_w128(a - 16 + HC_STRIDE, u32x4{z, z, z, d[0] << 16}); \
          lds_w128(a - 32 + 2 * HC_STRIDE, u32x4{z, z, z, z}); lds_w128(a - 16 + 2 * HC_STRIDE, u32x4{z, z, z, d[0]}); \
          lds_w128(a - 32 + 3 * HC_STRIDE, u32x4{z, z, z, z}); lds_w128(a - 16 + 3 * HC_STRIDE, u32x4{z, z, d[0] << 16, a01}); } } while (0)
#define H_PIN() asm volatile("" : "+v"(hd[0]), "+v"(hd[1]), "+v"(he[0]), "+v"(he[1]))
      u32 touch = 0;
      u32x2 f1 = u32x2{0u, 0u};
      const u32 f1o = WS_F1 + (xcd * 32 + (lane & 31)) * FSTRIDE;
      bool xr_issued = false;
#define GET_TAG() do { const u32x2 tg = lds_r64v(lds0 + L_TAG); tag = u32x2{(u32)__builtin_amdgcn_readfirstlane(tg[0]), (u32)__builtin_amdgcn_readfirstlane(tg[1])}; } while (0)
#define XR_ISSUE() do { UNR for (int i = 0; i < 2; ++i) { const int j = aw + 8 * i, c = j >> 2, p = j & 3; \
        if (j < 12) glds16_sc1(ws + WS_XR + (size_t)((c < 2 ? ch0 + c : ch2) * 4096 + p * 1024 + lane * 16), lds0 + L_XR + c * XR_CH + p * 1024); } } while (0)
      asm volatile("s_barrier" ::: "memory");
      {
        const int lid = idx * 512 + t;
        const char* tp;
        if (lid < 5120) tp = (const char*)x + (size_t)jr * 512 * 1280 + (size_t)lid * 128;
        else { const int l2 = lid - 5120; int row = l2 >> 4, c = l2 & 15; row = row > 639 ? 639 : row; c = (jc == 1 && c > 13) ? 13 : c;
               tp = (const char*)wqkv + (size_t)row * 3840 + jc * 2048 + c * 128; }
        touch = *(const u32*)tp;
      }
      SBAR();
      if (t < 256) { H_WRITE(0, 0); H_WRITE(1, 2); } else H_WRITE(0, 1);
      if (has_gemm) {
        const char* xs = (const char*)x + (size_t)(rt * 128 + aw * 16 + (lane >> 3)) * 1280 + (((lane & 7) ^ (lane >> 3)) * 16);
        const char* ws0 = (const char*)wqkv + (size_t)(aw * 8 + g) * 3840 + ct * 256 + ((l15 ^ ((g << 2) | ((2 * aw) & 3))) * 16);
        const char* ws1 = (const char*)wqkv + (size_t)(aw * 8 + 4 + g) * 3840 + ct * 256 + ((l15 ^ ((g << 2) | ((2 * aw + 1) & 3))) * 16);
        const u32 xd = lds0 + aw * 2048, wd = lds0 + 16384 + aw * 2048;
#define L_ISSUE(kt) do { const u32 sb_ = ((kt) % 3) * L_STAGE; \
          glds16(xs + (kt) * 128, xd + sb_); glds16(ws0 + (size_t)(kt) * 64 * 3840, wd + sb_); \
          glds16(xs + 8 * 1280 + (kt) * 128, xd + sb_ + 1024); glds16(ws1 + (size_t)(kt) * 64 * 3840, wd + sb_ + 1024); } while (0)
        L_ISSUE(0); L_ISSUE(1);
        STAMP(1);
        VM_BAR(4); STAMP(2); CSTAMP(13); L_ISSUE(2);
        VM_BAR(4); L_ISSUE(3);
        VM_BAR(4); L_ISSUE(4);
        VM_BAR(4); L_ISSUE(5);
        VM_BAR(4); STAMP(3); L_ISSUE(6);
        VM_BAR(4); L_ISSUE(7);
        if (wid == 8) {
          const char* f1p = ws + f1o;
          asm volatile("global_load_dwordx2 %0, %1, off sc1\n\ts_waitcnt vmcnt(5) lgkmcnt(0)\n\ts_barrier" : "=v"(f1) : "v"(f1p) : "memory");
          L_ISSUE(8);
          asm volatile("s_waitcnt vmcnt(4)" : "+v"(f1) :: "memory");
          GET_TAG();
          if (flags_ok(f1, tag, true)) cnt_sig(lds0, C_S2);
          LG_BAR();
        } else {
          VM_BAR(4); L_ISSUE(8);
          VM_BAR(4);
        }
        L_ISSUE(9);
        xr_issued = __builtin_amdgcn_readfirstlane(*(const volatile LDS3 u32*)(uintptr_t)(lds0 + L_CNT + 4 * C_S2)) != 0;
        if (xr_issued) {
          XR_ISSUE();
          if (aw < 4) { VM_BAR(6); VM_BAR(2); } else { VM_BAR(5); VM_BAR(1); }
        } else { VM_BAR(4); VM_BAR(0); }
        STAMP(12); CSTAMP(14);
      } else {
        UNR for (int i = 0; i < 10; ++i) LG_BAR();
      }
      asm volatile("" :: "v"(touch));
      GET_TAG();
      if (!xr_issued) {
        cnt_wait(lds0, C_B, 8);
        if (wid == 8) { flags_wait(wr, f1o, true, tag); cnt_sig(lds0, C_S2); }
        cnt_wait(lds0, C_S2, 1);
        XR_ISSUE();
      }
      VM0();
      cnt_sig(lds0, C_XR);
    }
    STAMP(4);
    FRESH_IDS();
    {
      const int cw = wid - 8;
      const int c = cw < 4 ? (cw >> 1) : 2;
      const int chn = c < 2 ? ch0 + c : ch2;
      const float dcoef = bf_lo((u32)ssmD[chn]);
      const u32 xb = lds0 + L_XR + c * XR_CH;
      const u32 ha = lds0 + L_HC + c * HC_CH + ((l15 + 1) & 3) * HC_STRIDE + (8 * g + ((l15 + 1) & ~3)) * 2;
      const u32 xa = xb + 480 - 32 * l15 + 16 * g;
      const u32 yrow = WS_Y2 + chn * 4096;
      f32x4 acc[8];
      UNR for (int i = 0; i < 8; ++i) acc[i] = f32x4{0.f, 0.f, 0.f, 0.f};
      cnt_wait(lds0, C_XR, 8);
      STAMP(5);
      if (cw < 4) {
        const int hf = cw & 1;
        conv_accum<0xFF, 4>(ha + hf * 256, xa + hf * 256, acc);
        STAMP(6);
        __builtin_amdgcn_s_setprio(1);
        const u32 red = lds0 + L_RED1 + c * 8192 + lane * 16;
        cnt_wait(lds0, C_B, 8);
        if (hf == 0) { UNR for (int i = 0; i < 4; ++i) lds_w128(red + i * 1024, __builtin_bit_cast(u32x4, acc[4 + i])); }
        else { UNR for (int i = 0; i < 4; ++i) lds_w128(red + 4096 + i * 1024, __builtin_bit_cast(u32x4, acc[i])); }
        cnt_add(lds0, C_P0 + c);
        cnt_wait(lds0, C_P0 + c, 2);
        u32x2 y[4];
        if (hf == 0) { UNR for (int i = 0; i < 4; ++i) y[i] = conv_fin(acc[i] + __builtin_bit_cast(f32x4, lds_r128(red + 4096 + i * 1024)), i, xb, dcoef, l15, g); }
        else { UNR for (int i = 0; i < 4; ++i) y[i] = conv_fin(acc[4 + i] + __builtin_bit_cast(f32x4, lds_r128(red + i * 1024)), 4 + i, xb, dcoef, l15, g); }
        conv_store2(wr, yrow, y[0], 4 * hf, y[1], 4 * hf + 1, l15, g);
        conv_store2(wr, yrow, y[2], 4 * hf + 2, y[3], 4 * hf + 3, l15, g);
      } else {
        const int qr = cw - 4;
        const u32 red = lds0 + L_RED2 + lane * 16;
        if (!odd) {
          conv_accum<0xC4, 2>(ha + qr * 128, xa + qr * 128, acc);
          STAMP(6);
          __builtin_amdgcn_s_setprio(1);
          cnt_wait(lds0, C_B, 8);
          const int tl[3] = {7, 6, 2};
          UNR for (int j = 0; j < 3; ++j) if (qr != j) lds_w128(red + (j * 3 + (qr > j ? qr - 1 : qr)) * 1024, __builtin_bit_cast(u32x4, acc[tl[j]]));
          cnt_add(lds0, C_Q4);
          cnt_wait(lds0, C_Q4, 4);
          UNR for (int j = 0; j < 3; ++j) if (qr == j) {
            f32x4 a = acc[tl[j]];
            UNR for (int w = 0; w < 3; ++w) a += __builtin_bit_cast(f32x4, lds_r128(red + (j * 3 + w) * 1024));
            conv_store1(wr, yrow, conv_fin(a, tl[j], xb, dcoef, l15, g), tl[j], l15, g);
          }
        } else {
          conv_accum<0x3B, 2>(ha + qr * 128, xa + qr * 128, acc);
          STAMP(6);
          __builtin_amdgcn_s_setprio(1);
          cnt_wait(lds0, C_B, 8);
          const int tl[5] = {5, 4, 3, 1, 0};
          UNR for (int j = 0; j < 5; ++j) { const int ow = j & 3; if (qr != ow) lds_w128(red + (j * 3 + (qr > ow ? qr - 1 : qr)) * 1024, __builtin_bit_cast(u32x4, acc[tl[j]])); }
          cnt_add(lds0, C_Q4);
          cnt_wait(lds0, C_Q4, 4);
          UNR for (int j = 0; j < 5; ++j) if (qr == (j & 3)) {
            f32x4 a = acc[tl[j]];
            UNR for (int w = 0; w < 3; ++w) a += __builtin_bit_cast(f32x4, lds_r128(red + (j * 3 + w) * 1024));
            conv_store1(wr, yrow, conv_fin(a, tl[j], xb, dcoef, l15, g), tl[j], l15, g);
          }
        }
      }
    }
    STAMP(7);
    __builtin_amdgcn_s_setprio(0);
    o_clear(wr, WS_O + (qb * 16 + (wid - 8) * 2) * 20480, head);
    VM0();
    if (cnt_add(lds0, C_Y) == 7) {
      flag_publish(wr, WS_F3 + (xcd * 32 + idx) * FSTRIDE, tag);
      flag_publish(wr, WS_F4 + (qb * 32 + head) * FSTRIDE, tag);
    }
    cnt_wait<4>(lds0, C_Y, 8);
    STAMP(8);
    FRESH_IDS();
    {
      const int aw = wid - 8, cb = idx >> 2;
      const int rg = lane / 10, ck = lane - rg * 10;
      u32 woff[2];
      {
        const int r = aw * 4 + (lane < 40 ? rg : 0), gg = 2 * (r >> 4) + ((r >> 2) & 1), j = 4 * ((r >> 3) & 1) + (r & 3);
        woff[0] = (20 * (gg >> 1) + 8 * (gg & 1) + j) * 1280 + cb * 160 + ck * 16;
        woff[1] = (40 * gg + (j < 4 ? 16 + j : 32 + j)) * 1280 + cb * 160 + ck * 16;
        asm volatile("" : "+v"(woff[0]), "+v"(woff[1]));
      }
      cnt_wait<4>(lds0, C_KV, 8);
      if (lane < 40) {
        UNR for (int ks = 0; ks < 16; ++ks) glds16_sb(woff[0], (const char*)wo + ks * 40 * 1280, lds0 + L_WO + ks * 5120 + aw * 640);
        UNR for (int ks = 0; ks < 4; ++ks) glds16_sb(woff[1], (const char*)wo + ks * 160 * 1280, lds0 + L_WO + (16 + ks) * 5120 + aw * 640);
      }
      VM0();
      cnt_sig(lds0, C_W);
      if (b == 0 && wid == 15) {
        _Pragma("unroll 1") for (u32 spins = 0; spins < SPIN_CAP; ++spins) {
          asm volatile("" ::: "memory");
          bool ok = true;
          UNR for (int j = 0; j < 4; ++j) { const u32x2 v = ld64_sc1(wr, WS_F1 + (j * 64 + lane) * FSTRIDE); ok = ok && v[0] == tag[0] && v[1] == tag[1]; }
          if (__all(ok)) break;
          __builtin_amdgcn_s_sleep(POLL_SLEEP);
        }
        flag_publish(wr, WS_EPOCH, tag);
      }
      if (wid == 9) {
        flags_wait(wr, WS_F4 + (xcd * 32 + (lane & 31)) * FSTRIDE, true, tag);
        cnt_sig(lds0, C_TAG);
      }
      cnt_wait<8>(lds0, C_O, GATE_N);
    }
    {
      const int aw = wid - 8, t = tid - 512;
      const int rgp = aw & 3, cs = aw >> 2;
      const int row = idx * 64 + rgp * 16 + l15;
      const int ck0 = cs * 4 + g, ck1 = 8 + (g & 1);
      const u32x4 q0 = *(const u32x4*)(qin + (size_t)row * 640 + xcd * 80 + ck0 * 8);
      const u32x4 q1 = *(const u32x4*)(qin + (size_t)row * 640 + xcd * 80 + ck1 * 8);
      if (wid == 8) {
        flags_wait(wr, WS_F3 + (xcd * 32 + (lane & 31)) * FSTRIDE, true, tag);
        cnt_add(lds0, C_YOK);
      }
      cnt_wait<2>(lds0, C_YOK, 1);
      STAMP(9);
      const u32x4 y0 = ld128_sc1(wr, WS_Y2 + (xcd * 80 + (t >> 3)) * 4096 + idx * 128 + (t & 7) * 16);
      const int t2 = 512 + (t & 127);
      const u32x4 y1 = ld128_sc1(wr, WS_Y2 + (xcd * 80 + (t2 >> 3)) * 4096 + idx * 128 + (t2 & 7) * 16);
      lds_w128(lds0 + L_GT + (t >> 3) * 144 + (t & 7) * 16, y0);
      if (t < 128) lds_w128(lds0 + L_GT + (t2 >> 3) * 144 + (t2 & 7) * 16, y1);
      cnt_add(lds0, C_G);
      cnt_wait(lds0, C_G, 8);
      STAMP(10);
      const u32 ga = lds0 + L_GT + (l15 >> 2) * 144 + (rgp * 16 + 4 * (lane & 3)) * 2;
      u32x2 a0 = lds_tr(ga + ck0 * 8 * 144), a1 = lds_tr(ga + ck0 * 8 * 144 + 4 * 144);
      u32x2 b0 = lds_tr(ga + ck1 * 8 * 144), b1 = lds_tr(ga + ck1 * 8 * 144 + 4 * 144);
      asm volatile("" : "+v"(a0), "+v"(a1), "+v"(b0), "+v"(b1));
      out128(orr, (row * 1280 + xcd * 80 + ck0 * 8) * 2,
          u32x4{pack2(bf_lo(q0[0]) * bf_lo(a0[0]), bf_hi(q0[0]) * bf_hi(a0[0])), pack2(bf_lo(q0[1]) * bf_lo(a0[1]), bf_hi(q0[1]) * bf_hi(a0[1])),
                pack2(bf_lo(q0[2]) * bf_lo(a1[0]), bf_hi(q0[2]) * bf_hi(a1[0])), pack2(bf_lo(q0[3]) * bf_lo(a1[1]), bf_hi(q0[3]) * bf_hi(a1[1]))});
      if (cs == 0 && g < 2)
        out128(orr, (row * 1280 + xcd * 80 + ck1 * 8) * 2,
            u32x4{pack2(bf_lo(q1[0]) * bf_lo(b0[0]), bf_hi(q1[0]) * bf_hi(b0[0])), pack2(bf_lo(q1[1]) * bf_lo(b0[1]), bf_hi(q1[1]) * bf_hi(b0[1])),
                  pack2(bf_lo(q1[2]) * bf_lo(b1[0]), bf_hi(q1[2]) * bf_hi(b1[0])), pack2(bf_lo(q1[3]) * bf_lo(b1[1]), bf_hi(q1[3]) * bf_hi(b1[1]))});
    }
    __builtin_amdgcn_s_setprio(0);
    STAMP(11);
  }
  STAMP(15);
  STAMP_FLUSH(ws);
  asm volatile(".Lpf_end:");
}

extern "C" void kernel_launch(void* const* d_in, const int* in_sizes, int n_in, void* d_out, int out_size, void* d_ws, size_t ws_size,
                              hipStream_t stream) {
  fused_kernel<<<256, 1024, 0, stream>>>((const u16*)d_in[0], (const u16*)d_in[1], (const u16*)d_in[2], (const u16*)d_in[3], (const u16*)d_in[4],
                                         (const u16*)d_in[5], (const u16*)d_in[6], (const u16*)d_in[7], (const u16*)d_in[8], (char*)d_ws, (u16*)d_out);
}
```

```cpp
#include <hip/hip_runtime.h>
#include <hip/hip_bf16.h>
#include <stdint.h>

typedef unsigned short u16;
typedef unsigned int u32;
typedef unsigned long long u64;
typedef __bf16 bf16x8 __attribute__((ext_vector_type(8)));
typedef __bf16 bf16x2 __attribute__((ext_vector_type(2)));
typedef float f32x4 __attribute__((ext_vector_type(4)));
typedef float f32x2 __attribute__((ext_vector_type(2)));
typedef u32 u32x2 __attribute__((ext_vector_type(2)));
typedef u32 u32x4 __attribute__((ext_vector_type(4)));
typedef short s16x4 __attribute__((ext_vector_type(4)));
typedef __amdgpu_buffer_rsrc_t rsrc_t;
#define LDS3 __attribute__((address_space(3)))
#define DI __device__ __forceinline__
#define UNR _Pragma("unroll")
#define SBAR() __builtin_amdgcn_sched_barrier(0)

constexpr u32 PLANE = 2048u * 640u * 2u;
constexpr u32 WS_XR = 0;
constexpr u32 WS_Q = 1 * PLANE;
constexpr u32 WS_K = 2 * PLANE;
constexpr u32 WS_V = 3 * PLANE;
constexpr u32 WS_O = 4 * PLANE;
constexpr u32 WS_Y2 = 5 * PLANE;
constexpr u32 WS_EPOCH = 6 * PLANE;
constexpr u32 FSTRIDE = 128;
constexpr u32 WS_F1 = WS_EPOCH + 2048, WS_F2 = WS_F1 + 320 * FSTRIDE, WS_F3 = WS_F2 + 256 * FSTRIDE, WS_F4 = WS_F3 + 256 * FSTRIDE;
constexpr u32 WS_END = WS_F4 + 256 * FSTRIDE;
constexpr float QSCALE = 0.22360679774997896f * 1.4426950408889634f;

constexpr u32 L_STAGE = 32768, L_KV = 65536, L_HC = 98304, HC_STRIDE = 4160, HC_CH = 16640, L_XR = 148224, XR_CH = 4608, L_CNT = 162048, LDS_BYTES = 162304;
constexpr u32 L_AK = 0, L_AV = 24576, L_ONES = 45056, L_RED1 = 49152, L_RED2 = 65536;
constexpr u32 L_TAG = L_CNT + 240;
constexpr u32 L_WO = 45312, L_GT = 148224, L_RED3 = 0;
enum { C_B = 0, C_QKV, C_KV, C_O, C_OOK, C_XR, C_P0, C_P1, C_Q4, C_Y, C_YOK, C_G, C_W, C_R0, C_R1, C_R2, C_R3, C_S1, C_S2, C_H, C_TAG,
       C_NUM = 24 };
constexpr int F1_T = 1;
constexpr u32 SENT = 0xffffffffu;
constexpr u32 SPIN_CAP = 1u << 19;
constexpr int POLL_SLEEP = 4;

DI u32 nosent(u32 v) { return v == SENT ? 0x7fc07fc0u : v; }
DI float bf_lo(u32 v) { return __uint_as_float(v << 16); }
DI float bf_hi(u32 v) { return __uint_as_float(v & 0xffff0000u); }
DI u32 pack2(float lo, float hi) { f32x2 v = {lo, hi}; return __builtin_bit_cast(u32, __builtin_convertvector(v, bf16x2)); }
DI f32x4 mfma16(u32x4 a, u32x4 b, f32x4 c) {
  return __builtin_amdgcn_mfma_f32_16x16x32_bf16(__builtin_bit_cast(bf16x8, a), __builtin_bit_cast(bf16x8, b), c, 0, 0, 0);
}
DI void glds16(const void* g, u32 lds_base) {
  u32 sv;
  asm volatile("s_mov_b32 %0, m0\n\ts_mov_b32 m0, %2\n\ts_nop 0\n\tglobal_load_lds_dwordx4 %1, off\n\ts_mov_b32 m0, %0"
               : "=&s"(sv) : "v"(g), "s"(lds_base) : "memory");
}
DI void glds16_sb(u32 voff, const void* sbase, u32 lds_base) {
  u32 sv;
  asm volatile("s_mov_b32 %0, m0\n\ts_mov_b32 m0, %3\n\ts_nop 0\n\tglobal_load_lds_dwordx4 %1, %2\n\ts_mov_b32 m0, %0"
               : "=&s"(sv) : "v"(voff), "s"(sbase), "s"(lds_base) : "memory");
}
DI void glds16_sc1(const void* g, u32 lds_base) {
  u32 sv;
  asm volatile("s_mov_b32 %0, m0\n\ts_mov_b32 m0, %2\n\ts_nop 0\n\tglobal_load_lds_dwordx4 %1, off sc1\n\ts_mov_b32 m0, %0"
               : "=&s"(sv) : "v"(g), "s"(lds_base) : "memory");
}
DI u32x4 lds_r128(u32 a) { return *(const LDS3 u32x4*)(uintptr_t)a; }
DI u32x2 lds_r64(u32 a) { return *(const LDS3 u32x2*)(uintptr_t)a; }
DI u32x2 lds_r64v(u32 a) { return *(const volatile LDS3 u32x2*)(uintptr_t)a; }
DI void lds_w128(u32 a, u32x4 v) { *(LDS3 u32x4*)(uintptr_t)a = v; }
DI u32x2 lds_tr(u32 a) { return __builtin_bit_cast(u32x2, __builtin_amdgcn_ds_read_tr16_b64_v4i16((LDS3 s16x4*)(uintptr_t)a)); }
DI void st128_sc1(rsrc_t r, u32 off, u32x4 v) { __builtin_amdgcn_raw_buffer_store_b128(v, r, off, 0, 16); }
DI void st64_sc1(rsrc_t r, u32 off, u32x2 v) { __builtin_amdgcn_raw_buffer_store_b64(v, r, off, 0, 16); }
DI u32x4 ld128_sc1(rsrc_t r, u32 off) { return __builtin_amdgcn_raw_buffer_load_b128(r, off, 0, 16); }
DI u32x2 ld64_sc1(rsrc_t r, u32 off) { return __builtin_amdgcn_raw_buffer_load_b64(r, off, 0, 16); }
#define VM_BAR(N) asm volatile("s_waitcnt vmcnt(" #N ") lgkmcnt(0)\n\ts_barrier" ::: "memory")
#define LG_BAR() asm volatile("s_waitcnt lgkmcnt(0)\n\ts_barrier" ::: "memory")
#define VM0() asm volatile("s_waitcnt vmcnt(0)" ::: "memory")

DI u32 cnt_add(u32 lds0, int c) {
  asm volatile("s_waitcnt lgkmcnt(0)" ::: "memory");
  u32 old = 0;
  if ((threadIdx.x & 63) == 0) old = __hip_atomic_fetch_add((LDS3 u32*)(uintptr_t)(lds0 + L_CNT + 4 * c), 1u, __ATOMIC_RELAXED, __HIP_MEMORY_SCOPE_WORKGROUP);
  return __builtin_amdgcn_readfirstlane(old);
}
DI void cnt_sig(u32 lds0, int c) {
  asm volatile("s_waitcnt lgkmcnt(0)" ::: "memory");
  if ((threadIdx.x & 63) == 0) (void)__hip_atomic_fetch_add((LDS3 u32*)(uintptr_t)(lds0 + L_CNT + 4 * c), 1u, __ATOMIC_RELAXED, __HIP_MEMORY_SCOPE_WORKGROUP);
  asm volatile("" ::: "memory");
}
template <int SLP = 1>
DI void cnt_wait(u32 lds0, int c, u32 n) {
  _Pragma("unroll 1") for (u32 spins = 0; spins < SPIN_CAP; ++spins) {
    if (__builtin_amdgcn_readfirstlane(__hip_atomic_load((LDS3 u32*)(uintptr_t)(lds0 + L_CNT + 4 * c), __ATOMIC_RELAXED, __HIP_MEMORY_SCOPE_WORKGROUP)) >= n) break;
    __builtin_amdgcn_s_sleep(SLP);
  }
  asm volatile("" ::: "memory");
}
DI void flag_publish(rsrc_t r, u32 off, u32x2 tag) { if ((threadIdx.x & 63) == 0) st64_sc1(r, off, tag); }
DI bool flags_ok(u32x2 v, u32x2 tag, bool need) { return __all(!need || (v[0] == tag[0] && v[1] == tag[1])); }
DI void flags_wait(rsrc_t r, u32 off, bool need, u32x2 tag) {
  _Pragma("unroll 1") for (u32 spins = 0; spins < SPIN_CAP; ++spins) {
    asm volatile("" ::: "memory");
    const u32x2 v = ld64_sc1(r, off);
    if (flags_ok(v, tag, need)) break;
    __builtin_amdgcn_s_sleep(POLL_SLEEP);
  }
  asm volatile("" ::: "memory");
}
DI void out128(rsrc_t r, u32 off, u32x4 v) { __builtin_amdgcn_raw_buffer_store_b128(v, r, off, 0, 2); }
DI void out64(rsrc_t r, u32 off, u32x2 v) { __builtin_amdgcn_raw_buffer_store_b64(v, r, off, 0, 2); }
DI u32x4 swap_pack(u32 a0, u32 a1, u32 b0, u32 b1) {
  auto s0 = __builtin_amdgcn_permlane16_swap(a0, b0, false, false);
  auto s1 = __builtin_amdgcn_permlane16_swap(a1, b1, false, false);
  return u32x4{s0[0], s1[0], s0[1], s1[1]};
}
DI float xmax4(float m) {
  auto r = __builtin_amdgcn_permlane32_swap(__float_as_uint(m), __float_as_uint(m), false, false);
  m = fmaxf(__uint_as_float(r[0]), __uint_as_float(r[1]));
  auto s = __builtin_amdgcn_permlane16_swap(__float_as_uint(m), __float_as_uint(m), false, false);
  return fmaxf(__uint_as_float(s[0]), __uint_as_float(s[1]));
}
DI float ex2(float v) { return __builtin_amdgcn_exp2f(v); }

DI void o_clear(rsrc_t wr, u32 obase, int head) {
  int lane = threadIdx.x & 63; asm volatile("" : "+v"(lane));
  const int l15 = lane & 15, g = lane >> 4;
  st128_sc1(wr, obase + (g & 1) * 20480 + (2 * head + (g >> 1)) * 256 + l15 * 16, u32x4{SENT, SENT, SENT, SENT});
  if (lane < 16) st128_sc1(wr, obase + (lane >> 3) * 20480 + 16384 + head * 128 + (lane & 7) * 16, u32x4{SENT, SENT, SENT, SENT});
}
template <bool SWAP>
DI void gemm1_compute(u32 lds0, rsrc_t wr, int wid, int lane, int rt, int ct, u32 f1off, u32x2 tag) {
  asm volatile("" : "+v"(lane));
  const int l15 = lane & 15, g = lane >> 4, wm = wid >> 2, wn = wid & 3;
  const u32 xa0 = lds0 + (wm * 64 + l15) * 128 + ((g ^ (lane & 7)) * 16), xa1 = xa0 ^ 64;
  u32 wa[2][2];
  {
    const int q = l15 >> 2, p = lane & 3;
    UNR for (int nt = 0; nt < 2; ++nt) UNR for (int t = 0; t < 2; ++t) {
      const int swz = (q << 2) | ((2 * g + t) & 3);
      wa[nt][t] = lds0 + 16384 + 256 * (8 * g + 4 * t + q) + 16 * ((2 * (wn * 2 + nt) + (p >> 1)) ^ swz) + 8 * (p & 1);
    }
  }
  f32x4 acc[4][2];
  UNR for (int i = 0; i < 4; ++i) UNR for (int j = 0; j < 2; ++j) acc[i][j] = f32x4{0.f, 0.f, 0.f, 0.f};
  u32x4 xf[2][4], wf[2][2];
#define G1_RW(set, kt, ks, nt) do { const u32 sb_ = ((kt) % 3) * L_STAGE; \
    const u32x2 lo = lds_tr(wa[nt][0] + sb_ + (ks) * 8192), hi = lds_tr(wa[nt][1] + sb_ + (ks) * 8192); wf[set][nt] = u32x4{lo[0], lo[1], hi[0], hi[1]}; } while (0)
#define G1_RX(set, kt, ks, mt) xf[set][mt] = lds_r128(((ks) ? xa1 : xa0) + ((kt) % 3) * L_STAGE + (mt) * 2048)
#define G1_M(set, mt, nt) acc[mt][nt] = SWAP ? mfma16(wf[set][nt], xf[set][mt], acc[mt][nt]) : mfma16(xf[set][mt], wf[set][nt], acc[mt][nt])
#define G1_HALF(cs, ns, kt, ks, RD) do { \
    G1_M(cs, 0, 0); if (RD) G1_RW(ns, kt, ks, 0); SBAR(); \
    G1_M(cs, 0, 1); if (RD) { G1_RX(ns, kt, ks, 0); G1_RX(ns, kt, ks, 1); } SBAR(); \
    G1_M(cs, 1, 0); if (RD) G1_RW(ns, kt, ks, 1); SBAR(); \
    G1_M(cs, 1, 1); if (RD) G1_RX(ns, kt, ks, 2); SBAR(); \
    G1_M(cs, 2, 0); if (RD) G1_RX(ns, kt, ks, 3); SBAR(); \
    G1_M(cs, 2, 1); G1_M(cs, 3, 0); G1_M(cs, 3, 1); SBAR(); } while (0)
  G1_RW(0, 0, 0, 0); G1_RX(0, 0, 0, 0); G1_RW(0, 0, 0, 1); G1_RX(0, 0, 0, 1); G1_RX(0, 0, 0, 2); G1_RX(0, 0, 0, 3);
  SBAR();
  UNR for (int t = 0; t < 10; ++t) {
    G1_HALF(0, 1, t, 1, true);
    if (t < 9) {
      if (t == F1_T) VM_BAR(0); else LG_BAR();
      if (t == F1_T && wid == 0) flag_publish(wr, f1off, tag);
      SBAR();
    }
    G1_HALF(1, 0, t + 1, 0, t < 9);
  }
  if (SWAP) {
    const float sc = ct < 5 ? QSCALE : 1.0f;
    const u32 base = ct < 5 ? WS_Q : WS_K;
    UNR for (int nt = 0; nt < 2; ++nt) {
      const int G8 = ((ct % 5) * 128 + wn * 32 + nt * 16) / 8 + (g >> 1);
      const int pr = G8 / 5, gr = G8 - pr * 5;
      UNR for (int mp = 0; mp < 2; ++mp) {
        const f32x4 a = acc[2 * mp][nt], b = acc[2 * mp + 1][nt];
        const u32x4 v = swap_pack(pack2(a[0] * sc, a[1] * sc), pack2(a[2] * sc, a[3] * sc), pack2(b[0] * sc, b[1] * sc), pack2(b[2] * sc, b[3] * sc));
        const int tile = rt * 8 + wm * 4 + 2 * mp + (g & 1);
        st128_sc1(wr, base + ((pr * 128 + tile) * 5 + gr) * 256 + l15 * 16, v);
      }
    }
  } else {
    UNR for (int nt = 0; nt < 2; ++nt) {
      const int c = (ct - 10) * 128 + wn * 32 + nt * 16 + l15;
      const int h = c / 20, d = c - h * 20;
      const int off = d < 16 ? g * 256 + d * 16 : 1024 + g * 64 + (d - 16) * 16;
      UNR for (int mp = 0; mp < 2; ++mp) {
        const int blk = rt * 4 + wm * 2 + mp;
        const f32x4 a = acc[2 * mp][nt], b = acc[2 * mp + 1][nt];
        st128_sc1(wr, WS_V + (h * 64 + blk) * 1280 + off, u32x4{pack2(a[0], a[1]), pack2(a[2], a[3]), pack2(b[0], b[1]), pack2(b[2], b[3])});
      }
    }
  }
}

struct AttnOut { f32x4 oA0, oA1, oB0, oB1; };

DI void attn_slow(u32 ka, u32 va0, u32 va1, u32 v1inc, u32x4 qA, u32x4 qB, int kb0, int l15, int g, AttnOut& o) {
  const f32x4 z = f32x4{0.f, 0.f, 0.f, 0.f};
  float tA = -INFINITY, tB = -INFINITY;
  for (int t = 2 * kb0; t < 18; ++t) {
    const u32x4 kf = lds_r128(ka + t * 768);
    const f32x4 sA = mfma16(kf, qA, z), sB = mfma16(kf, qB, z);
    UNR for (int r = 0; r < 4; ++r) {
      const int dA = 16 * t + 4 * g + r - l15;
      tA = fmaxf(tA, (dA >= 1 && dA <= 256) ? sA[r] : -INFINITY);
      tB = fmaxf(tB, (dA >= 17 && dA <= 272) ? sB[r] : -INFINITY);
    }
  }
  const float mA = xmax4(tA), mB = xmax4(tB);
  o.oA0 = o.oA1 = o.oB0 = o.oB1 = z;
  for (int kbk = kb0; kbk < 9; ++kbk) {
    float pa[8], pb[8];
    UNR for (int hf = 0; hf < 2; ++hf) {
      const int t = 2 * kbk + hf;
      const u32x4 kf = lds_r128(ka + t * 768);
      const f32x4 sA = mfma16(kf, qA, z), sB = mfma16(kf, qB, z);
      UNR for (int r = 0; r < 4; ++r) {
        const int dA = 16 * t + 4 * g + r - l15;
        pa[4 * hf + r] = (dA >= 1 && dA <= 256) ? ex2(sA[r] - mA) : 0.f;
        pb[4 * hf + r] = (dA >= 17 && dA <= 272) ? ex2(sB[r] - mB) : 0.f;
      }
    }
    const u32x4 vf0 = lds_r128(va0 + kbk * 1280), vf1 = lds_r128(va1 + kbk * v1inc);
    const u32x4 pA = u32x4{pack2(pa[0], pa[1]), pack2(pa[2], pa[3]), pack2(pa[4], pa[5]), pack2(pa[6], pa[7])};
    const u32x4 pB = u32x4{pack2(pb[0], pb[1]), pack2(pb[2], pb[3]), pack2(pb[4], pb[5]), pack2(pb[6], pb[7])};
    o.oA0 = mfma16(vf0, pA, o.oA0); o.oA1 = mfma16(vf1, pA, o.oA1);
    o.oB0 = mfma16(vf0, pB, o.oB0); o.oB1 = mfma16(vf1, pB, o.oB1);
  }
}

DI void attn_fast(u32 ka, u32 va0, u32 va1, u32 v1inc, u32x4 qA, u32x4 qB, int l15, int g, AttnOut& o) {
  const f32x4 z = f32x4{0.f, 0.f, 0.f, 0.f};
  bool lo[4];
  UNR for (int r = 0; r < 4; ++r) lo[r] = 4 * g + r <= l15;
  u32x4 kf[2][2], vf[2][2], pA, pB;
  f32x4 S[2][4];
  kf[1][0] = lds_r128(ka + 16 * 768); kf[1][1] = lds_r128(ka + 17 * 768);
  kf[0][0] = lds_r128(ka); kf[0][1] = lds_r128(ka + 768);
  vf[1][0] = lds_r128(va0 + 8 * 1280); vf[1][1] = lds_r128(va1 + 8 * v1inc);
  f32x4 cA, cB;
  {
    const f32x4 sA = mfma16(kf[1][0], qA, z), sB = mfma16(kf[1][1], qB, z), sB0 = mfma16(kf[1][0], qB, z);
    SBAR();
    kf[1][0] = lds_r128(ka + 2 * 768); kf[1][1] = lds_r128(ka + 3 * 768);
    float mA = -INFINITY, mB = -INFINITY;
    UNR for (int r = 0; r < 4; ++r) { mA = fmaxf(mA, lo[r] ? sA[r] : -INFINITY); mB = fmaxf(mB, lo[r] ? sB[r] : -INFINITY); }
    mA = xmax4(mA); mB = xmax4(mB);
    cA = f32x4{-mA, -mA, -mA, -mA}; cB = f32x4{-mB, -mB, -mB, -mB};
    S[0][0] = mfma16(kf[0][0], qA, cA); S[0][1] = mfma16(kf[0][1], qA, cA); S[0][3] = mfma16(kf[0][1], qB, cB);
    float a[4], b0[4], b1[4];
    UNR for (int r = 0; r < 4; ++r) { a[r] = lo[r] ? ex2(sA[r] - mA) : 0.f; b0[r] = ex2(sB0[r] - mB); b1[r] = lo[r] ? ex2(sB[r] - mB) : 0.f; }
    pA = u32x4{pack2(a[0], a[1]), pack2(a[2], a[3]), 0u, 0u};
    pB = u32x4{pack2(b0[0], b0[1]), pack2(b0[2], b0[3]), pack2(b1[0], b1[1]), pack2(b1[2], b1[3])};
  }
  o.oA0 = o.oA1 = o.oB0 = o.oB1 = z;
  SBAR();
  UNR for (int k = 0; k < 8; ++k) {
    const int c = k & 1, n = c ^ 1;
    vf[c][0] = lds_r128(va0 + k * 1280); vf[c][1] = lds_r128(va1 + k * v1inc);
    if (k < 6) { kf[c][0] = lds_r128(ka + (2 * k + 4) * 768); kf[c][1] = lds_r128(ka + (2 * k + 5) * 768); }
    SBAR();
    u32 np[8];
    UNR for (int i = 0; i < 4; ++i) {
      if (k < 7) S[n][i] = mfma16(kf[n][i & 1], i < 2 ? qA : qB, i < 2 ? cA : cB);
      float e[4];
      if (k == 0 && i == 2) { np[4] = 0u; np[5] = 0u; }
      else {
        e[0] = ex2(S[c][i][0]); e[1] = ex2(S[c][i][1]);
      }
      if (i == 0) o.oA0 = mfma16(vf[n][0], pA, o.oA0);
      if (i == 1) o.oA1 = mfma16(vf[n][1], pA, o.oA1);
      if (i == 2) o.oB0 = mfma16(vf[n][0], pB, o.oB0);
      if (i == 3) o.oB1 = mfma16(vf[n][1], pB, o.oB1);
      if (!(k == 0 && i == 2)) {
        e[2] = ex2(S[c][i][2]); e[3] = ex2(S[c][i][3]);
        if (k == 0 && (i == 0 || i == 3)) { UNR for (int r = 0; r < 4; ++r) e[r] = lo[r] ? 0.f : e[r]; }
        np[2 * i] = pack2(e[0], e[1]); np[2 * i + 1] = pack2(e[2], e[3]);
      }
      SBAR();
    }
    pA = u32x4{np[0], np[1], np[2], np[3]}; pB = u32x4{np[4], np[5], np[6], np[7]};
  }
  o.oA0 = mfma16(vf[1][0], pA, o.oA0); o.oA1 = mfma16(vf[1][1], pA, o.oA1);
  o.oB0 = mfma16(vf[1][0], pB, o.oB0); o.oB1 = mfma16(vf[1][1], pB, o.oB1);
}

template <int TMASK, int NR>
DI void conv_accum(u32 ha, u32 xa, f32x4 (&acc)[8]) {
  constexpr int NF = (TMASK & 0xC0) ? 8 : 6;
  u32x4 A[8], B[8];
#define CV_LA(i, rr) do { if ((i) < NF) { const u32x2 lo_ = lds_r64v(ha + (rr) * 64 + (i) * 512), hi_ = lds_r64v(ha + (rr) * 64 + (i) * 512 + 8); \
    A[i] = u32x4{lo_[0], lo_[1], hi_[0], hi_[1]}; } } while (0)
#define CV_LB(m, rr) do { if ((m) < NF) B[m] = lds_r128(xa + (rr) * 64 + (7 - (m)) * 512); } while (0)
#define CV_MM(i, m) do { if ((i) < NF && (m) < NF && (i) + (m) < 8 && ((TMASK >> ((i) + (m))) & 1)) acc[(i) + (m)] = mfma16(A[i], B[m], acc[(i) + (m)]); } while (0)
  CV_LB(0, 0); UNR for (int i = 7; i >= 4; --i) CV_LA(i, 0);
  UNR for (int m = 1; m < 4; ++m) CV_LB(m, 0);
  UNR for (int i = 0; i < 4; ++i) { CV_LA(i, 0); CV_LB(7 - i, 0); }
  SBAR();
  UNR for (int rr = 0; rr < NR; ++rr) {
    UNR for (int m = 0; m < 4; ++m) UNR for (int i = 7; i >= 4; --i) CV_MM(i, m);
    SBAR();
    if (rr + 1 < NR) { UNR for (int i = 7; i >= 4; --i) CV_LA(i, rr + 1); }
    SBAR();
    UNR for (int i = 0; i < 4; ++i) UNR for (int m = 7; m >= 4; --m) CV_MM(i, m);
    SBAR();
    if (rr + 1 < NR) { UNR for (int m = 7; m >= 4; --m) CV_LB(m, rr + 1); }
    SBAR();
    UNR for (int m = 0; m < 4; ++m) {
      UNR for (int i = 0; i < 4; ++i) CV_MM(i, m);
      SBAR();
      if (rr + 1 < NR) CV_LB(m, rr + 1);
      SBAR();
    }
    if (rr + 1 < NR) { UNR for (int i = 0; i < 4; ++i) CV_LA(i, rr + 1); }
    SBAR();
  }
}
DI u32x2 conv_fin(f32x4 a, int n, u32 xbase, float dcoef, int l15, int g) {
  const int s = 256 * n + 16 * l15 + 4 * g;
  const u32x2 xv = lds_r64(xbase + (2044 - s) * 2);
  const u32 y01 = pack2(a[0], a[1]), y23 = pack2(a[2], a[3]);
  const u32 d01 = pack2(bf_hi(xv[1]) * dcoef, bf_lo(xv[1]) * dcoef), d23 = pack2(bf_hi(xv[0]) * dcoef, bf_lo(xv[0]) * dcoef);
  return u32x2{pack2(bf_lo(y01) + bf_lo(d01), bf_hi(y01) + bf_hi(d01)), pack2(bf_lo(y23) + bf_lo(d23), bf_hi(y23) + bf_hi(d23))};
}
DI void conv_store2(rsrc_t wr, u32 rowoff, u32x2 ya, int na, u32x2 yb, int nb, int l15, int g) {
  const u32x4 v = swap_pack(ya[0], ya[1], yb[0], yb[1]);
  st128_sc1(wr, rowoff + (256 * ((g & 1) ? nb : na) + 16 * l15 + 8 * (g >> 1)) * 2, v);
}
DI void conv_store1(rsrc_t wr, u32 rowoff, u32x2 ya, int na, int l15, int g) { st64_sc1(wr, rowoff + (256 * na + 16 * l15 + 4 * g) * 2, ya); }

DI u32 kv_pair(u32 k0, u32 k1, u32 k2, u32 k3, u32 v, u32 sa01, u32 sa23, u32 sb01, u32 sb23) {
  u32 p = pack2(bf_lo(k0) * bf_hi(sa23), bf_hi(k0) * bf_hi(sb23));
  u32 q = pack2(bf_lo(k1) * bf_lo(sa23), bf_hi(k1) * bf_lo(sb23));
  p = pack2(bf_lo(p) + bf_lo(q), bf_hi(p) + bf_hi(q));
  q = pack2(bf_lo(k2) * bf_hi(sa01), bf_hi(k2) * bf_hi(sb01));
  p = pack2(bf_lo(p) + bf_lo(q), bf_hi(p) + bf_hi(q));
  q = pack2(bf_lo(k3) * bf_lo(sa01), bf_hi(k3) * bf_lo(sb01));
  p = pack2(bf_lo(p) + bf_lo(q), bf_hi(p) + bf_hi(q));
  return pack2(bf_lo(p) * bf_lo(v), bf_hi(p) * bf_hi(v));
}
DI u32 lo2(u32 a, u32 b) { return __builtin_amdgcn_perm(b, a, 0x05040100u); }
DI u32 hi2(u32 a, u32 b) { return __builtin_amdgcn_perm(b, a, 0x07060302u); }

__global__ __launch_bounds__(1024, 4) void fused_kernel(const u16* __restrict__ qin, const u16* __restrict__ kin, const u16* __restrict__ vin,
                                                         const u16* __restrict__ shk, const u16* __restrict__ ssmk, const u16* __restrict__ ssmD,
                                                         const u16* __restrict__ x, const u16* __restrict__ wqkv, const u16* __restrict__ wo,
                                                         char* __restrict__ ws, u16* __restrict__ out) {
  __shared__ __attribute__((aligned(1024))) char lds[LDS_BYTES];
  const u32 lds0 = (u32)(uintptr_t)lds;
  int tid = threadIdx.x, lane = tid & 63;
  const int wid = __builtin_amdgcn_readfirstlane(tid >> 6);
  int l15 = lane & 15, g = lane >> 4;
#define FRESH_IDS() do { asm volatile("" : "+v"(tid)); lane = tid & 63; l15 = lane & 15; g = lane >> 4; } while (0)
  const int b = blockIdx.x, xcd = b & 7, idx = b >> 3;
  const rsrc_t wr = __builtin_amdgcn_make_buffer_rsrc(ws, 0, WS_END, 0x00020000);
  const rsrc_t orr = __builtin_amdgcn_make_buffer_rsrc(out, 0, 2048 * 1280 * 2, 0x00020000);
  u32 code_touch = 0;
  if (wid == 6) {
    u64 pc; u32 len;
    asm volatile("s_getpc_b64 %0\n.Lpf_here:\n\ts_mov_b32 %1, .Lpf_end-.Lpf_here" : "=s"(pc), "=s"(len));
    const u32 off = (u32)(idx * 16 + (lane & 15)) * 128u;
    if (lane < 16) code_touch = *(const __attribute__((address_space(1))) u32*)(pc + (off + 4 <= len ? off : 0u));
  }
  u32x2 tag;
  const int jr = xcd >> 1, jc = xcd & 1;
  const bool has_gemm = !(jc == 1 && idx >= 28);
  const int rt = jr * 4 + (idx & 3), ct = jc * 8 + (idx >> 2);
  const int cbase = 80 * xcd + 5 * (idx >> 1), odd = idx & 1;
  const int ch0 = cbase + (odd ? 3 : 0), ch2 = cbase + 2;

  const int head = xcd * 4 + (idx >> 3), qb = idx & 7, pr = head >> 1, g0 = 2 * (head & 1);
  if (wid < 8) {
    {
      const int it = tid < 320 ? tid : tid - 192;
      const int cq = it % 20, rg = it / 20;
      const int s0 = idx * 64 + rg * 4, c0 = xcd * 80 + cq * 4;
      u32x2 kk[7], vv[4];
      u32x4 sh0, sh1;
      if (wid < 5) {
        UNR for (int j = 0; j < 7; ++j) { const int r = s0 - 3 + j; kk[j] = *((const u32x2*)(kin + (size_t)(r < 0 ? 0 : r) * 640 + c0)); }
        UNR for (int j = 0; j < 4; ++j) vv[j] = *((const u32x2*)(vin + (size_t)(s0 + j) * 640 + c0));
        sh0 = *(const u32x4*)(shk + c0 * 4); sh1 = *(const u32x4*)(shk + c0 * 4 + 8);
      }
      if (tid >= 320 && tid < 320 + C_NUM) *(LDS3 u32*)(uintptr_t)(lds0 + L_CNT + (tid - 320) * 4) = 0u;
      if (tid >= 384 && tid < 480) lds_w128(lds0 + L_XR + ((tid - 384) >> 5) * XR_CH + 4096 + (tid & 31) * 16, u32x4{0u, 0u, 0u, 0u});
      LG_BAR();
      if (wid < 5) {
        if (s0 == 0) { kk[0] = u32x2{0u, 0u}; kk[1] = u32x2{0u, 0u}; kk[2] = u32x2{0u, 0u}; }
        u32 r01[4], r23[4];
        UNR for (int j = 0; j < 4; ++j) {
          r01[j] = kv_pair(kk[j][0], kk[j + 1][0], kk[j + 2][0], kk[j + 3][0], vv[j][0], sh0[0], sh0[1], sh0[2], sh0[3]);
          r23[j] = kv_pair(kk[j][1], kk[j + 1][1], kk[j + 2][1], kk[j + 3][1], vv[j][1], sh1[0], sh1[1], sh1[2], sh1[3]);
        }
        const u32 a = lds0 + L_KV + (cq * 4) * 144 + (60 - 4 * rg) * 2;
        *(LDS3 u32x2*)(uintptr_t)(a) = u32x2{lo2(r01[3], r01[2]), lo2(r01[1], r01[0])};
        *(LDS3 u32x2*)(uintptr_t)(a + 144) = u32x2{hi2(r01[3], r01[2]), hi2(r01[1], r01[0])};
        *(LDS3 u32x2*)(uintptr_t)(a + 288) = u32x2{lo2(r23[3], r23[2]), lo2(r23[1], r23[0])};
        *(LDS3 u32x2*)(uintptr_t)(a + 432) = u32x2{hi2(r23[3], r23[2]), hi2(r23[1], r23[0])};
      }
      if (wid == 7) {
        u64 e;
        asm volatile("s_load_dwordx2 %0, %1, 0x0\n\ts_waitcnt lgkmcnt(0)" : "=s"(e) : "s"(ws + WS_EPOCH) : "memory");
        e += 1;
        if (lane == 0) *(LDS3 u32x2*)(uintptr_t)(lds0 + L_TAG) = u32x2{(u32)e, (u32)(e >> 32)};
      }
      asm volatile("" :: "v"(code_touch));
      cnt_sig(lds0, C_S1); cnt_wait(lds0, C_S1, 8);
      st128_sc1(wr, WS_XR + (xcd * 80 + (tid >> 3)) * 4096 + (1984 - 64 * idx) * 2 + (tid & 7) * 16, lds_r128(lds0 + L_KV + (tid >> 3) * 144 + (tid & 7) * 16));
      if (tid < 128) st128_sc1(wr, WS_XR + (xcd * 80 + 64 + (tid >> 3)) * 4096 + (1984 - 64 * idx) * 2 + (tid & 7) * 16,
                               lds_r128(lds0 + L_KV + (64 + (tid >> 3)) * 144 + (tid & 7) * 16));
      { const u32x2 tg = lds_r64v(lds0 + L_TAG); tag = u32x2{(u32)__builtin_amdgcn_readfirstlane(tg[0]), (u32)__builtin_amdgcn_readfirstlane(tg[1])}; }
      LG_BAR();
      FRESH_IDS();
      if (has_gemm) {
        if (ct < 10) gemm1_compute<true>(lds0, wr, wid, lane, rt, ct, WS_F1 + (xcd * 32 + idx) * FSTRIDE, tag);
        else gemm1_compute<false>(lds0, wr, wid, lane, rt, ct, WS_F1 + (xcd * 32 + idx) * FSTRIDE, tag);
      } else {
        VM_BAR(0);
        if (wid == 0) flag_publish(wr, WS_F1 + (xcd * 32 + idx) * FSTRIDE, tag);
        UNR for (int t = 2; t < 10; ++t) LG_BAR();
      }
    }
    VM0();
    if (cnt_add(lds0, C_B) == 7) {
      if (has_gemm) flag_publish(wr, WS_F2 + (rt * 15 + ct) * FSTRIDE, tag);
    }
    FRESH_IDS();
    cnt_wait(lds0, C_B, 8);
    if (qb == 0) {
      lds_w128(lds0 + L_AK + tid * 16, u32x4{0u, 0u, 0u, 0u});
      if (tid < 256) lds_w128(lds0 + L_AK + 8192 + tid * 16, u32x4{0u, 0u, 0u, 0u});
      lds_w128(lds0 + L_AV + tid * 16, u32x4{0u, 0u, 0u, 0u});
      if (tid < 128) lds_w128(lds0 + L_AV + 8192 + tid * 16, u32x4{0u, 0u, 0u, 0u});
    }
    if (tid < 16) { const u32 o1 = (qb > 0 || tid >= 8) ? 0x3f803f80u : 0u; lds_w128(lds0 + L_ONES + tid * 16, u32x4{o1, o1, o1, o1}); }
    const char* ksrc[3];
    UNR for (int j = 0; j < 3; ++j) {
      const int s = 64 * j + lane, i = s / 48, u = s - i * 48;
      ksrc[j] = ws + WS_K + (((pr * 128 + qb * 16 - 16 + 4 * wid + i) * 5 + g0) * 256 + u * 16);
      asm volatile("" : "+v"(ksrc[j]));
    }
    const char* vsrc = ws + WS_V + ((head * 64 + qb * 8 - 8) * 1280 + wid * 1024 + lane * 16);
    asm volatile("" : "+v"(vsrc));
    if (wid == 0) {
      const int which = lane >> 3, r = (lane >> 1) & 3, hi = lane & 1;
      const int prt = 2 * qb - 2 + r, pct = 5 * which + ((20 * head + (hi ? 19 : 0)) >> 7);
      const bool need = lane < 24 && prt >= 0 && !(which == 0 && r < 2);
      flags_wait(wr, WS_F2 + (need ? (prt * 15 + pct) * FSTRIDE : 0), need, tag);
      cnt_add(lds0, C_QKV);
    }
    cnt_wait<2>(lds0, C_QKV, 1);
    if (qb > 0 || wid >= 4) { UNR for (int j = 0; j < 3; ++j) glds16_sc1(ksrc[j], lds0 + L_AK + (3 * wid + j) * 1024); }
    UNR for (int j = 0; j < 3; ++j) {
      const int n = wid + 8 * j;
      if (n < 20 && (qb > 0 || n >= 10)) glds16_sc1(vsrc + j * 8192, lds0 + L_AV + n * 1024);
    }
    const int qt = qb * 16 + wid * 2, gq = g < 3 ? g : 2;
    u32x4 qA = ld128_sc1(wr, WS_Q + ((pr * 128 + qt) * 5 + g0 + gq) * 256 + l15 * 16);
    u32x4 qB = ld128_sc1(wr, WS_Q + ((pr * 128 + qt + 1) * 5 + g0 + gq) * 256 + l15 * 16);
    {
      const bool zlo = g == 3 || ((head & 1) && g == 0), zhi = g == 3 || (!(head & 1) && g == 2);
      if (zlo) { qA[0] = 0u; qA[1] = 0u; qB[0] = 0u; qB[1] = 0u; }
      if (zhi) { qA[2] = 0u; qA[3] = 0u; qB[2] = 0u; qB[3] = 0u; }
    }
    VM0();
    cnt_sig(lds0, C_KV);
    cnt_wait(lds0, C_KV, 8);
    u32 tw;
    {
      const char* p1 = (const char*)wo + (size_t)(idx * 200 + (tid < 200 ? tid : 0)) * 128;
      const char* p2 = (const char*)qin + (size_t)(idx * 64 + ((tid & 127) >> 1)) * 1280 + xcd * 160 + (tid & 1) * 128;
      tw = *(const u32*)(tid < 256 ? p1 : p2);
    }
    {
      const u32 ka = lds0 + L_AK + wid * 1536 + gq * 256 + l15 * 16;
      const u32 va0 = lds0 + L_AV + wid * 1280 + g * 256 + l15 * 16;
      const u32 va1 = l15 < 4 ? lds0 + L_AV + wid * 1280 + 1024 + g * 64 + l15 * 16 : lds0 + L_ONES + wid * 16;
      const u32 v1inc = l15 < 4 ? 1280u : 16u;
      AttnOut o;
      attn_fast(ka, va0, va1, v1inc, qA, qB, l15, g, o);
      float lA = __shfl(o.oA1[0], 16 + l15), lB = __shfl(o.oB1[0], 16 + l15);
      if (__any(!(lA <= 16777216.f) || !(lB <= 16777216.f))) {
        attn_slow(ka, va0, va1, v1inc, qA, qB, qb == 0 ? 8 - wid : 0, l15, g, o);
        lA = __shfl(o.oA1[0], 16 + l15); lB = __shfl(o.oB1[0], 16 + l15);
      }
      asm volatile("" :: "v"(tw));
      const float iA = 1.0f / lA, iB = 1.0f / lB;
      cnt_wait(lds0, C_Y, 8);
      const u32x4 v = swap_pack(pack2(o.oA0[0] * iA, o.oA0[1] * iA), pack2(o.oA0[2] * iA, o.oA0[3] * iA),
                                pack2(o.oB0[0] * iB, o.oB0[1] * iB), pack2(o.oB0[2] * iB, o.oB0[3] * iB));
      st128_sc1(wr, WS_O + (qt + (g & 1)) * 20480 + (2 * head + (g >> 1)) * 256 + l15 * 16, u32x4{nosent(v[0]), nosent(v[1]), nosent(v[2]), nosent(v[3])});
      const u32x4 w = swap_pack(pack2(o.oA1[0] * iA, o.oA1[1] * iA), pack2(o.oA1[2] * iA, o.oA1[3] * iA),
                                pack2(o.oB1[0] * iB, o.oB1[1] * iB), pack2(o.oB1[2] * iB, o.oB1[3] * iB));
      if (g < 2) st64_sc1(wr, WS_O + (qt + g) * 20480 + 16384 + head * 128 + l15 * 8, u32x2{nosent(w[0]), nosent(w[1])});
    }
    cnt_sig(lds0, C_O);
    FRESH_IDS();
    const int rb = xcd * 4 + (idx & 3), cb = idx >> 2;
    {
      const int mt = wid & 3, kh = wid >> 2;
      const u32 wa = lds0 + L_WO + kh * 51200 + (16 * (g >> 1) + 4 * (g & 1) + (l15 >> 2)) * 160 + (lane & 3) * 8;
      const u32 ob = WS_O + (rb * 4 + mt) * 20480;
      const u32 o2 = ob + (kh ? 16384 + g * 256 + l15 * 8 : 6144 + lane * 16), o2d = kh ? 128u : 8u;
      f32x4 acc[5];
      u32x4 wf[2][5];
#define G2_RW(set, ks) do { UNR for (int j = 0; j < 5; ++j) { const u32x2 lo = lds_tr(wa + (ks) * 5120 + j * 32), hi = lds_tr(wa + (ks) * 5120 + 1280 + j * 32); \
        wf[set][j] = u32x4{lo[0], lo[1], hi[0], hi[1]}; } } while (0)
      cnt_wait<2>(lds0, C_W, 8);
      if (wid >= 4) __builtin_amdgcn_s_setprio(1);
      _Pragma("unroll 1") for (u32 spins = 0; spins < SPIN_CAP; ++spins) {
        G2_RW(0, 0);
        SBAR();
        if (wid == 4 && spins == 0) {
          cnt_wait<2>(lds0, C_TAG, 1);
          const int hh = lane & 31;
          const u32 ho = WS_O + (rb * 4 + 2 * (lane >> 5) + 1) * 20480 + 16384 + hh * 128 + 120;
          _Pragma("unroll 1") for (u32 sp = 0; sp < SPIN_CAP; ++sp) {
            asm volatile("" ::: "memory");
            const u32x2 v = ld64_sc1(wr, ho);
            if (__all(v[0] != SENT && v[1] != SENT)) break;
            __builtin_amdgcn_s_sleep(POLL_SLEEP);
          }
          cnt_sig(lds0, C_OOK);
        }
        cnt_wait<1>(lds0, C_OOK, 1);
        u32x4 of[10];
        UNR for (int ks = 0; ks < 6; ++ks) of[ks] = ld128_sc1(wr, ob + (kh * 10 + ks) * 1024 + lane * 16);
        UNR for (int ks = 6; ks < 10; ++ks) {
          const u32x2 lo = ld64_sc1(wr, o2 + (ks - 6) * 1024), hi = ld64_sc1(wr, o2 + (ks - 6) * 1024 + o2d);
          of[ks] = u32x4{lo[0], lo[1], hi[0], hi[1]};
        }
        SBAR();
        UNR for (int j = 0; j < 5; ++j) acc[j] = f32x4{0.f, 0.f, 0.f, 0.f};
        u32 mx = 0;
        UNR for (int ks = 0; ks < 10; ++ks) {
          UNR for (int j = 0; j < 5; ++j) {
            if (ks < 9) { const u32x2 lo = lds_tr(wa + (ks + 1) * 5120 + j * 32), hi = lds_tr(wa + (ks + 1) * 5120 + 1280 + j * 32);
                          wf[(ks + 1) & 1][j] = u32x4{lo[0], lo[1], hi[0], hi[1]}; }
            acc[j] = mfma16(wf[ks & 1][j], of[ks], acc[j]);
            SBAR();
          }
          mx = max(max(mx, of[ks][0]), of[ks][1]); mx = max(max(mx, of[ks][2]), of[ks][3]);
        }
        if (!__any(mx == SENT)) break;
      }
      cnt_wait(lds0, C_O, 8);
      const u32 red = lds0 + L_RED3 + mt * 5120 + lane * 16;
      if (kh == 0) { lds_w128(red + 2 * 1024, __builtin_bit_cast(u32x4, acc[2])); lds_w128(red + 3 * 1024, __builtin_bit_cast(u32x4, acc[3])); }
      else { lds_w128(red, __builtin_bit_cast(u32x4, acc[0])); lds_w128(red + 1024, __builtin_bit_cast(u32x4, acc[1])); lds_w128(red + 4 * 1024, __builtin_bit_cast(u32x4, acc[4])); }
      cnt_add(lds0, C_R0 + mt);
      cnt_wait(lds0, C_R0 + mt, 2);
      const u32 orow = ((rb * 64 + mt * 16 + l15) * 1280 + 640 + cb * 80) * 2;
      if (kh == 0) {
        const f32x4 a = acc[0] + __builtin_bit_cast(f32x4, lds_r128(red)), c = acc[1] + __builtin_bit_cast(f32x4, lds_r128(red + 1024));
        const f32x4 e = acc[4] + __builtin_bit_cast(f32x4, lds_r128(red + 4 * 1024));
        out128(orr, orow + ((g & 1) * 16 + (g >> 1) * 8) * 2, swap_pack(pack2(a[0], a[1]), pack2(a[2], a[3]), pack2(c[0], c[1]), pack2(c[2], c[3])));
        out64(orr, orow + (64 + 4 * g) * 2, u32x2{pack2(e[0], e[1]), pack2(e[2], e[3])});
      } else {
        const f32x4 a = acc[2] + __builtin_bit_cast(f32x4, lds_r128(red + 2 * 1024)), c = acc[3] + __builtin_bit_cast(f32x4, lds_r128(red + 3 * 1024));
        out128(orr, orow + (32 + (g & 1) * 16 + (g >> 1) * 8) * 2, swap_pack(pack2(a[0], a[1]), pack2(a[2], a[3]), pack2(c[0], c[1]), pack2(c[2], c[3])));
      }
    }
  } else {
    {
      const int aw = wid - 8, t = tid - 512, tc = t & 255;
      UNR for (int i = 0; i < 2; ++i) { const int j = aw + 8 * i, c = j >> 2, p = j & 3;
        if (j < 12) glds16((const char*)ssmk + (size_t)((c < 2 ? ch0 + c : ch2) * 4096 + p * 1024 + lane * 16), lds0 + L_HC + c * HC_CH + 32 + p * 1024); }
#define H_WRITE(c) do { const u32x4 d = lds_r128(lds0 + L_HC + (c) * HC_CH + 32 + tc * 16); const u32x2 e = lds_r64(lds0 + L_HC + (c) * HC_CH + 48 + tc * 16); \
        const u32 a01 = __builtin_amdgcn_alignbit(d[1], d[0], 16), a12 = __builtin_amdgcn_alignbit(d[2], d[1], 16), a23 = __builtin_amdgcn_alignbit(d[3], d[2], 16), \
                  a34 = __builtin_amdgcn_alignbit(e[0], d[3], 16), a45 = __builtin_amdgcn_alignbit(e[1], e[0], 16); \
        const u32 a = lds0 + L_HC + (c) * HC_CH + 32 + tc * 16; \
        lds_w128(a + HC_STRIDE, u32x4{a01, a12, a23, a34}); \
        lds_w128(a + 2 * HC_STRIDE, u32x4{d[1], d[2], d[3], e[0]}); lds_w128(a + 3 * HC_STRIDE, u32x4{a12, a23, a34, a45}); \
        if (tc == 0) { const u32 z = 0u; \
          lds_w128(a - 32, u32x4{z, z, z, z}); lds_w128(a - 16, u32x4{z, z, z, z}); \
          lds_w128(a - 32 + HC_STRIDE, u32x4{z, z, z, z}); lds_w128(a - 16 + HC_STRIDE, u32x4{z, z, z, d[0] << 16}); \
          lds_w128(a - 32 + 2 * HC_STRIDE, u32x4{z, z, z, z}); lds_w128(a - 16 + 2 * HC_STRIDE, u32x4{z, z, z, d[0]}); \
          lds_w128(a - 32 + 3 * HC_STRIDE, u32x4{z, z, z, z}); lds_w128(a - 16 + 3 * HC_STRIDE, u32x4{z, z, d[0] << 16, a01}); } } while (0)
      u32x2 f1 = u32x2{0u, 0u};
      const u32 f1o = WS_F1 + (xcd * 32 + (lane & 31)) * FSTRIDE;
      bool xr_issued = false;
#define GET_TAG() do { const u32x2 tg = lds_r64v(lds0 + L_TAG); tag = u32x2{(u32)__builtin_amdgcn_readfirstlane(tg[0]), (u32)__builtin_amdgcn_readfirstlane(tg[1])}; } while (0)
#define XR_ISSUE() do { UNR for (int i = 0; i < 2; ++i) { const int j = aw + 8 * i, c = j >> 2, p = j & 3; \
        if (j < 12) glds16_sc1(ws + WS_XR + (size_t)((c < 2 ? ch0 + c : ch2) * 4096 + p * 1024 + lane * 16), lds0 + L_XR + c * XR_CH + p * 1024); } } while (0)
      asm volatile("s_barrier" ::: "memory");
      SBAR();
      if (has_gemm) {
        const char* xs = (const char*)x + (size_t)(rt * 128 + aw * 16 + (lane >> 3)) * 1280 + (((lane & 7) ^ (lane >> 3)) * 16);
        const char* ws0 = (const char*)wqkv + (size_t)(aw * 8 + g) * 3840 + ct * 256 + ((l15 ^ ((g << 2) | ((2 * aw) & 3))) * 16);
        const char* ws1 = (const char*)wqkv + (size_t)(aw * 8 + 4 + g) * 3840 + ct * 256 + ((l15 ^ ((g << 2) | ((2 * aw + 1) & 3))) * 16);
        const u32 xd = lds0 + aw * 2048, wd = lds0 + 16384 + aw * 2048;
        const int krot = ((((idx & 3) * 2 + (idx >> 2)) & 7) * 10) >> 3;
#define L_ISSUE(kt) do { const u32 sb_ = ((kt) % 3) * L_STAGE; int kk_ = (kt) + krot; kk_ = kk_ >= 10 ? kk_ - 10 : kk_; \
          glds16(xs + kk_ * 128, xd + sb_); glds16(ws0 + (size_t)kk_ * 64 * 3840, wd + sb_); \
          glds16(xs + 8 * 1280 + kk_ * 128, xd + sb_ + 1024); glds16(ws1 + (size_t)kk_ * 64 * 3840, wd + sb_ + 1024); } while (0)
        L_ISSUE(0); L_ISSUE(1);
        asm volatile("s_waitcnt vmcnt(8)" ::: "memory");
        cnt_sig(lds0, C_H); cnt_wait(lds0, C_H, 8);
        H_WRITE(t >> 8);
        if (t < 256) H_WRITE(2);
        VM_BAR(4); L_ISSUE(2);
        VM_BAR(4); L_ISSUE(3);
        VM_BAR(4); L_ISSUE(4);
        VM_BAR(4); L_ISSUE(5);
        VM_BAR(4); L_ISSUE(6);
        VM_BAR(4); L_ISSUE(7);
        if (wid == 8) {
          const char* f1p = ws + f1o;
          asm volatile("global_load_dwordx2 %0, %1, off sc1\n\ts_waitcnt vmcnt(5) lgkmcnt(0)\n\ts_barrier" : "=v"(f1) : "v"(f1p) : "memory");
          L_ISSUE(8);
          asm volatile("s_waitcnt vmcnt(4)" : "+v"(f1) :: "memory");
          GET_TAG();
          if (flags_ok(f1, tag, true)) cnt_sig(lds0, C_S2);
          LG_BAR();
        } else {
          VM_BAR(4); L_ISSUE(8);
          VM_BAR(4);
        }
        L_ISSUE(9);
        xr_issued = __builtin_amdgcn_readfirstlane(*(const volatile LDS3 u32*)(uintptr_t)(lds0 + L_CNT + 4 * C_S2)) != 0;
        if (xr_issued) {
          XR_ISSUE();
          if (aw < 4) { VM_BAR(6); VM_BAR(2); } else { VM_BAR(5); VM_BAR(1); }
        } else { VM_BAR(4); VM_BAR(0); }
      } else {
        VM_BAR(0);
        H_WRITE(t >> 8);
        if (t < 256) H_WRITE(2);
        UNR for (int i = 1; i < 10; ++i) LG_BAR();
      }
      GET_TAG();
      if (!xr_issued) {
        cnt_wait(lds0, C_B, 8);
        if (wid == 8) { flags_wait(wr, f1o, true, tag); cnt_sig(lds0, C_S2); }
        cnt_wait(lds0, C_S2, 1);
        XR_ISSUE();
      }
      VM0();
      cnt_sig(lds0, C_XR);
    }
    FRESH_IDS();
    {
      const int cw = wid - 8;
      const int c = cw < 4 ? (cw >> 1) : 2;
      const int chn = c < 2 ? ch0 + c : ch2;
      const float dcoef = bf_lo((u32)ssmD[chn]);
      const u32 xb = lds0 + L_XR + c * XR_CH;
      const u32 ha = lds0 + L_HC + c * HC_CH + ((l15 + 1) & 3) * HC_STRIDE + (8 * g + ((l15 + 1) & ~3)) * 2;
      const u32 xa = xb + 480 - 32 * l15 + 16 * g;
      const u32 yrow = WS_Y2 + chn * 4096;
      f32x4 acc[8];
      UNR for (int i = 0; i < 8; ++i) acc[i] = f32x4{0.f, 0.f, 0.f, 0.f};
      cnt_wait(lds0, C_XR, 8);
      if (cw < 4) {
        const int hf = cw & 1;
        conv_accum<0xFF, 4>(ha + hf * 256, xa + hf * 256, acc);
        __builtin_amdgcn_s_setprio(1);
        const u32 red = lds0 + L_RED1 + c * 8192 + lane * 16;
        cnt_wait(lds0, C_B, 8);
        if (hf == 0) { UNR for (int i = 0; i < 4; ++i) lds_w128(red + i * 1024, __builtin_bit_cast(u32x4, acc[4 + i])); }
        else { UNR for (int i = 0; i < 4; ++i) lds_w128(red + 4096 + i * 1024, __builtin_bit_cast(u32x4, acc[i])); }
        cnt_add(lds0, C_P0 + c);
        cnt_wait(lds0, C_P0 + c, 2);
        u32x2 y[4];
        if (hf == 0) { UNR for (int i = 0; i < 4; ++i) y[i] = conv_fin(acc[i] + __builtin_bit_cast(f32x4, lds_r128(red + 4096 + i * 1024)), i, xb, dcoef, l15, g); }
        else { UNR for (int i = 0; i < 4; ++i) y[i] = conv_fin(acc[4 + i] + __builtin_bit_cast(f32x4, lds_r128(red + i * 1024)), 4 + i, xb, dcoef, l15, g); }
        conv_store2(wr, yrow, y[0], 4 * hf, y[1], 4 * hf + 1, l15, g);
        conv_store2(wr, yrow, y[2], 4 * hf + 2, y[3], 4 * hf + 3, l15, g);
      } else {
        const int qr = cw - 4;
        const u32 red = lds0 + L_RED2 + lane * 16;
        if (!odd) {
          conv_accum<0xC4, 2>(ha + qr * 128, xa + qr * 128, acc);
          __builtin_amdgcn_s_setprio(1);
          cnt_wait(lds0, C_B, 8);
          const int tl[3] = {7, 6, 2};
          UNR for (int j = 0; j < 3; ++j) if (qr != j) lds_w128(red + (j * 3 + (qr > j ? qr - 1 : qr)) * 1024, __builtin_bit_cast(u32x4, acc[tl[j]]));
          cnt_add(lds0, C_Q4);
          cnt_wait(lds0, C_Q4, 4);
          UNR for (int j = 0; j < 3; ++j) if (qr == j) {
            f32x4 a = acc[tl[j]];
            UNR for (int w = 0; w < 3; ++w) a += __builtin_bit_cast(f32x4, lds_r128(red + (j * 3 + w) * 1024));
            conv_store1(wr, yrow, conv_fin(a, tl[j], xb, dcoef, l15, g), tl[j], l15, g);
          }
        } else {
          conv_accum<0x3B, 2>(ha + qr * 128, xa + qr * 128, acc);
          __builtin_amdgcn_s_setprio(1);
          cnt_wait(lds0, C_B, 8);
          const int tl[5] = {5, 4, 3, 1, 0};
          UNR for (int j = 0; j < 5; ++j) { const int ow = j & 3; if (qr != ow) lds_w128(red + (j * 3 + (qr > ow ? qr - 1 : qr)) * 1024, __builtin_bit_cast(u32x4, acc[tl[j]])); }
          cnt_add(lds0, C_Q4);
          cnt_wait(lds0, C_Q4, 4);
          UNR for (int j = 0; j < 5; ++j) if (qr == (j & 3)) {
            f32x4 a = acc[tl[j]];
            UNR for (int w = 0; w < 3; ++w) a += __builtin_bit_cast(f32x4, lds_r128(red + (j * 3 + w) * 1024));
            conv_store1(wr, yrow, conv_fin(a, tl[j], xb, dcoef, l15, g), tl[j], l15, g);
          }
        }
      }
    }
    __builtin_amdgcn_s_setprio(0);
    o_clear(wr, WS_O + (qb * 16 + (wid - 8) * 2) * 20480, head);
    VM0();
    if (cnt_add(lds0, C_Y) == 7) {
      flag_publish(wr, WS_F3 + (xcd * 32 + idx) * FSTRIDE, tag);
      flag_publish(wr, WS_F4 + (qb * 32 + head) * FSTRIDE, tag);
    }
    cnt_wait<4>(lds0, C_Y, 8);
    FRESH_IDS();
    {
      const int aw = wid - 8, cb = idx >> 2;
      const int rg = lane / 10, ck = lane - rg * 10;
      u32 woff[2];
      {
        const int r = aw * 4 + (lane < 40 ? rg : 0), gg = 2 * (r >> 4) + ((r >> 2) & 1), j = 4 * ((r >> 3) & 1) + (r & 3);
        woff[0] = (20 * (gg >> 1) + 8 * (gg & 1) + j) * 1280 + cb * 160 + ck * 16;
        woff[1] = (40 * gg + (j < 4 ? 16 + j : 32 + j)) * 1280 + cb * 160 + ck * 16;
        asm volatile("" : "+v"(woff[0]), "+v"(woff[1]));
      }
      cnt_wait<4>(lds0, C_KV, 8);
      if (lane < 40) {
        UNR for (int ks = 0; ks < 16; ++ks) glds16_sb(woff[0], (const char*)wo + ks * 40 * 1280, lds0 + L_WO + ks * 5120 + aw * 640);
        UNR for (int ks = 0; ks < 4; ++ks) glds16_sb(woff[1], (const char*)wo + ks * 160 * 1280, lds0 + L_WO + (16 + ks) * 5120 + aw * 640);
      }
      VM0();
      cnt_sig(lds0, C_W);
      if (b == 0 && wid == 15) {
        _Pragma("unroll 1") for (u32 spins = 0; spins < SPIN_CAP; ++spins) {
          asm volatile("" ::: "memory");
          bool ok = true;
          UNR for (int j = 0; j < 4; ++j) { const u32x2 v = ld64_sc1(wr, WS_F1 + (j * 64 + lane) * FSTRIDE); ok = ok && v[0] == tag[0] && v[1] == tag[1]; }
          if (__all(ok)) break;
          __builtin_amdgcn_s_sleep(POLL_SLEEP);
        }
        flag_publish(wr, WS_EPOCH, tag);
      }
      if (wid == 9) {
        flags_wait(wr, WS_F4 + (xcd * 32 + (lane & 31)) * FSTRIDE, true, tag);
        cnt_sig(lds0, C_TAG);
      }
    }
    {
      const int aw = wid - 8, t = tid - 512;
      const int rgp = aw & 3, cs = aw >> 2;
      const int row = idx * 64 + rgp * 16 + l15;
      const int ck0 = cs * 4 + g, ck1 = 8 + (g & 1);
      const u32x4 q0 = *(const u32x4*)(qin + (size_t)row * 640 + xcd * 80 + ck0 * 8);
      const u32x4 q1 = *(const u32x4*)(qin + (size_t)row * 640 + xcd * 80 + ck1 * 8);
      if (wid == 8) {
        flags_wait(wr, WS_F3 + (xcd * 32 + (lane & 31)) * FSTRIDE, true, tag);
        cnt_add(lds0, C_YOK);
      }
      cnt_wait<2>(lds0, C_YOK, 1);
      const u32x4 y0 = ld128_sc1(wr, WS_Y2 + (xcd * 80 + (t >> 3)) * 4096 + idx * 128 + (t & 7) * 16);
      const int t2 = 512 + (t & 127);
      const u32x4 y1 = ld128_sc1(wr, WS_Y2 + (xcd * 80 + (t2 >> 3)) * 4096 + idx * 128 + (t2 & 7) * 16);
      lds_w128(lds0 + L_GT + (t >> 3) * 144 + (t & 7) * 16, y0);
      if (t < 128) lds_w128(lds0 + L_GT + (t2 >> 3) * 144 + (t2 & 7) * 16, y1);
      cnt_add(lds0, C_G);
      cnt_wait(lds0, C_G, 8);
      const u32 ga = lds0 + L_GT + (l15 >> 2) * 144 + (rgp * 16 + 4 * (lane & 3)) * 2;
      u32x2 a0 = lds_tr(ga + ck0 * 8 * 144), a1 = lds_tr(ga + ck0 * 8 * 144 + 4 * 144);
      u32x2 b0 = lds_tr(ga + ck1 * 8 * 144), b1 = lds_tr(ga + ck1 * 8 * 144 + 4 * 144);
      asm volatile("" : "+v"(a0), "+v"(a1), "+v"(b0), "+v"(b1));
      out128(orr, (row * 1280 + xcd * 80 + ck0 * 8) * 2,
          u32x4{pack2(bf_lo(q0[0]) * bf_lo(a0[0]), bf_hi(q0[0]) * bf_hi(a0[0])), pack2(bf_lo(q0[1]) * bf_lo(a0[1]), bf_hi(q0[1]) * bf_hi(a0[1])),
                pack2(bf_lo(q0[2]) * bf_lo(a1[0]), bf_hi(q0[2]) * bf_hi(a1[0])), pack2(bf_lo(q0[3]) * bf_lo(a1[1]), bf_hi(q0[3]) * bf_hi(a1[1]))});
      if (cs == 0 && g < 2)
        out128(orr, (row * 1280 + xcd * 80 + ck1 * 8) * 2,
            u32x4{pack2(bf_lo(q1[0]) * bf_lo(b0[0]), bf_hi(q1[0]) * bf_hi(b0[0])), pack2(bf_lo(q1[1]) * bf_lo(b0[1]), bf_hi(q1[1]) * bf_hi(b0[1])),
                  pack2(bf_lo(q1[2]) * bf_lo(b1[0]), bf_hi(q1[2]) * bf_hi(b1[0])), pack2(bf_lo(q1[3]) * bf_lo(b1[1]), bf_hi(q1[3]) * bf_hi(b1[1]))});
    }
    __builtin_amdgcn_s_setprio(0);
  }
  asm volatile(".Lpf_end:");
}

extern "C" void kernel_launch(void* const* d_in, const int* in_sizes, int n_in, void* d_out, int out_size, void* d_ws, size_t ws_size,
                              hipStream_t stream) {
  fused_kernel<<<256, 1024, 0, stream>>>((const u16*)d_in[0], (const u16*)d_in[1], (const u16*)d_in[2], (const u16*)d_in[3], (const u16*)d_in[4],
                                         (const u16*)d_in[5], (const u16*)d_in[6], (const u16*)d_in[7], (const u16*)d_in[8], (char*)d_ws, (u16*)d_out);
}
```
